```python
import torch

def _softmax(x, axis=-1):
    return torch.softmax(x, dim=axis)
def _gelu(x):
    return torch.nn.functional.gelu(x, approximate="tanh")
def _silu(x):
    return torch.nn.functional.silu(x)
def _sigmoid(x):
    return torch.sigmoid(x)
def _layernorm(x, axis=-1, eps=1e-5):
    m = x.mean(dim=axis, keepdim=True)
    v = x.var(dim=axis, keepdim=True, unbiased=False)
    return (x - m) / torch.sqrt(v + eps)
def _scatter_add(tgt, idx, src, axis=0):
    return tgt.clone().index_add_(axis, idx, src)
def _segment_sum(src, seg, n):
    out = torch.zeros((n,) + src.shape[1:], dtype=src.dtype)
    return out.index_add_(0, seg, src)
def _pad(x, pw):
    flat = tuple(v for pair in reversed(pw) for v in pair)
    return torch.nn.functional.pad(x, flat)

kernel_name = 'standalone__geglu_ffn__prompt'

def setup_inputs():

    _w = (lambda a: (lambda t: t.to(torch.bfloat16) if t.is_floating_point() else t)(torch.as_tensor(a)))
    B, S, D, F = 1, 2048, 1024, 3584
    return {
        "x": _w((lambda *s: torch.randn(*s, dtype=torch.bfloat16) * 0.05)(B, S, D)),
        "w_gate": _w((lambda *s: torch.randn(*s, dtype=torch.bfloat16) * 0.05)(D, F)),
        "w_up": _w((lambda *s: torch.randn(*s, dtype=torch.bfloat16) * 0.05)(D, F)),
        "w_down": _w((lambda *s: torch.randn(*s, dtype=torch.bfloat16) * 0.05)(F, D)),
    }

def reference(x, w_gate, w_up, w_down):
    return (_gelu(x @ w_gate) * (x @ w_up)) @ w_down
```

```cpp
#include <hip/hip_runtime.h>
#include <hip/hip_bf16.h>
#include <stdint.h>

typedef __bf16 bf16x8 __attribute__((ext_vector_type(8)));
typedef __bf16 bf16x4 __attribute__((ext_vector_type(4)));
typedef float f32x4 __attribute__((ext_vector_type(4)));
typedef float f32x2 __attribute__((ext_vector_type(2)));
typedef unsigned u32x2 __attribute__((ext_vector_type(2)));
typedef unsigned u32x4 __attribute__((ext_vector_type(4)));
#define LAS __attribute__((address_space(3)))

#define S_ 2048
#define D_ 1024
#define F_ 3584

__device__ __forceinline__ void glds16(const void* sbase, uint32_t voff, uint32_t lds_addr) {
  asm volatile("s_mov_b32 m0, %2\n\ts_nop 0\n\tglobal_load_lds_dwordx4 %0, %1"
               :: "v"(voff), "s"(sbase), "s"(lds_addr) : "memory");
}
template <int OFF>
__device__ __forceinline__ void glds16_off(const void* sbase, uint32_t voff, uint32_t lds_addr) {
  asm volatile("s_mov_b32 m0, %2\n\ts_nop 0\n\tglobal_load_lds_dwordx4 %0, %1 offset:%3"
               :: "v"(voff), "s"(sbase), "s"(lds_addr), "n"(OFF) : "memory");
}
#define WAIT_VM(n) asm volatile("s_waitcnt vmcnt(" #n ")" ::: "memory")
#define WAIT_LGKM0() asm volatile("s_waitcnt lgkmcnt(0)" ::: "memory")
#define BAR() do { asm volatile("" ::: "memory"); __builtin_amdgcn_s_barrier(); asm volatile("" ::: "memory"); } while (0)
#define SCHED() __builtin_amdgcn_sched_barrier(0)

__device__ __forceinline__ bf16x8 lds_b128(uint32_t addr) { return *(LAS bf16x8*)(addr); }
__device__ __forceinline__ bf16x8 lds_tr2(uint32_t a0, uint32_t a1) {
  bf16x4 w0 = __builtin_amdgcn_ds_read_tr16_b64_v4bf16((LAS bf16x4*)(a0));
  bf16x4 w1 = __builtin_amdgcn_ds_read_tr16_b64_v4bf16((LAS bf16x4*)(a1));
  return __builtin_shufflevector(w0, w1, 0, 1, 2, 3, 4, 5, 6, 7);
}
__device__ __forceinline__ f32x4 mfma16(bf16x8 a, bf16x8 b, f32x4 c) {
  return __builtin_amdgcn_mfma_f32_16x16x32_bf16(a, b, c, 0, 0, 0);
}
__device__ __forceinline__ float bf16r(float v) {
  return (float)((__bf16)v);
}
__device__ __forceinline__ unsigned pack2(float lo, float hi) {
  typedef __bf16 bf16x2 __attribute__((ext_vector_type(2)));
  bf16x2 p; p.x = (__bf16)lo; p.y = (__bf16)hi;
  return __builtin_bit_cast(unsigned, p);
}
__device__ __forceinline__ float gelu_tanh(float x) {
  const float c1 = -2.0f * 0.7978845608028654f * 1.4426950408889634f;
  const float c2 = c1 * 0.044715f;
  float x2 = x * x;
  float p = __builtin_fmaf(x2, c2, c1);
  float e = __builtin_amdgcn_exp2f(x * p);
  return x * __builtin_amdgcn_rcpf(1.0f + e);
}

constexpr int G1_XB = 32768;
constexpr int G1_WB = 28672;
constexpr int G1_WOFF = 2 * G1_XB;
constexpr int G1_LDS = 2 * G1_XB + 2 * G1_WB;
constexpr int G1_NT = D_ / 64;

struct G1Ctx {
  const char* xs;
  const char* ws[4];
  uint32_t xv[2];
  uint32_t wv[4];
  uint32_t xl;
  uint32_t wl[4];
  uint32_t xa[2];
  uint32_t wa[2];
};

template <int NWL>
__device__ __forceinline__ void g1_issue_one(const G1Ctx& c, int idx, int t, int buf) {
  if (idx < 4) {
    glds16(c.xs + (idx >> 1) * (16 * 2048) + t * 128, c.xv[idx & 1], c.xl + buf * G1_XB + idx * 1024);
  } else {
    int j = idx - 4;
    glds16(c.ws[j] + (size_t)t * (64 * F_ * 2), c.wv[j], c.wl[j] + buf * G1_WB);
  }
}

template <int NF, int B, bool HAS_NEXT, bool HAS_NEXT2, bool ISSUE_CUR>
__device__ __forceinline__ void g1_tile(const G1Ctx& c, int t, f32x4 (&acc)[NF][4], bf16x8 (&xf)[2][4], bf16x8 (&wf)[4]) {
  constexpr int NWL = (NF == 8) ? 3 : 4;
  constexpr int NL = 4 + NWL;
  constexpr int NS = 2 * NF;
#pragma unroll
  for (int j = 0; j < NS; ++j) {
    const int s = j / NF, f = j % NF;
    if (j == NS - 2 && HAS_NEXT) {
      WAIT_LGKM0();
      WAIT_VM(0);
      BAR();
    }
    if (j + 2 < NS) {
      const int s2 = (j + 2) / NF, f2 = (j + 2) % NF;
      const uint32_t o = B * G1_WB + s2 * (32 * 224) + (f2 & 1) * 14336 + (f2 >> 1) * 32;
      wf[(j + 2) & 3] = lds_tr2(c.wa[0] + o, c.wa[1] + o);
    } else if (HAS_NEXT) {
      const int f2 = j + 2 - NS;
      const uint32_t o = (B ^ 1) * G1_WB + (f2 & 1) * 14336 + (f2 >> 1) * 32;
      wf[(j + 2) & 3] = lds_tr2(c.wa[0] + o, c.wa[1] + o);
    }
    if (j < 4) xf[1][j] = lds_b128(c.xa[1] + B * G1_XB + j * 2048);
    if (j == NS - 2 && HAS_NEXT) {
#pragma unroll
      for (int m = 0; m < 4; ++m) xf[0][m] = lds_b128(c.xa[0] + (B ^ 1) * G1_XB + m * 2048);
    }
    if (j >= NS - 2 && HAS_NEXT2) g1_issue_one<NWL>(c, j - (NS - 2), t + 2, B);
    if (ISSUE_CUR && j + 2 < NL) g1_issue_one<NWL>(c, j + 2, t + 1, B ^ 1);
#pragma unroll
    for (int m = 0; m < 4; ++m) acc[f][m] = mfma16(wf[j & 3], xf[s][m], acc[f][m]);
    SCHED();
  }
}

template <int NF>
__device__ __forceinline__ void g1_run(const G1Ctx& c, unsigned char* Hf, int brow, int wr, int nfg0, int lane) {
  constexpr int NWL = (NF == 8) ? 3 : 4;
  constexpr int NL = 4 + NWL;
  f32x4 acc[NF][4];
#pragma unroll
  for (int f = 0; f < NF; ++f)
#pragma unroll
    for (int m = 0; m < 4; ++m) acc[f][m] = (f32x4){0.f, 0.f, 0.f, 0.f};
  bf16x8 xf[2][4], wf[4];
#pragma unroll
  for (int i = 0; i < NL; ++i) g1_issue_one<NWL>(c, i, 0, 0);
#pragma unroll
  for (int i = 0; i < NL; ++i) g1_issue_one<NWL>(c, i, 1, 1);
  if (NL == 7) WAIT_VM(7); else WAIT_VM(8);
  BAR();
#pragma unroll
  for (int m = 0; m < 4; ++m) xf[0][m] = lds_b128(c.xa[0] + m * 2048);
  wf[0] = lds_tr2(c.wa[0], c.wa[1]);
  wf[1] = lds_tr2(c.wa[0] + 14336, c.wa[1] + 14336);
  SCHED();
  g1_tile<NF, 0, true, true, false>(c, 0, acc, xf, wf);
  for (int t = 1; t < G1_NT - 3; t += 2) {
    g1_tile<NF, 1, true, true, true>(c, t, acc, xf, wf);
    g1_tile<NF, 0, true, true, true>(c, t + 1, acc, xf, wf);
  }
  g1_tile<NF, 1, true, true, true>(c, G1_NT - 3, acc, xf, wf);
  g1_tile<NF, 0, true, false, true>(c, G1_NT - 2, acc, xf, wf);
  g1_tile<NF, 1, false, false, false>(c, G1_NT - 1, acc, xf, wf);

#pragma unroll
  for (int i = 0; i < NF / 2; ++i) {
    const int nfg = nfg0 + i;
    const int kb = nfg >> 1, half = nfg & 1;
#pragma unroll
    for (int m = 0; m < 4; ++m) {
      const int mfrag = (brow >> 4) + wr * 4 + m;
      const int rp = mfrag >> 3, mfl = mfrag & 7;
      f32x4 g = acc[2 * i][m], u = acc[2 * i + 1][m];
      float h[4];
#pragma unroll
      for (int r = 0; r < 4; ++r) h[r] = bf16r(gelu_tanh(bf16r(g[r]))) * bf16r(u[r]);
      u32x2 o; o.x = pack2(h[0], h[1]); o.y = pack2(h[2], h[3]);
      *(u32x2*)(Hf + ((size_t)((rp * 112 + kb) * 8 + mfl)) * 1024 + lane * 16 + half * 8) = o;
    }
  }
}

__global__ __launch_bounds__(512, 2) void gemm1_kernel(const unsigned char* __restrict__ x, const unsigned char* __restrict__ wg,
                                                       const unsigned char* __restrict__ wu, unsigned char* __restrict__ Hf) {
  __shared__ __attribute__((aligned(1024))) unsigned char smem[G1_LDS];
  const uint32_t lbase = (uint32_t)(uintptr_t)(LAS unsigned char*)smem;
  const int bid = blockIdx.x;
  const int xcd = bid & 7, idx = bid >> 3;
  const int pn = xcd * 4 + (idx & 3), pm = idx >> 2;
  const int brow = pm * 256;
  const int lane = threadIdx.x & 63;
  const int wid = __builtin_amdgcn_readfirstlane(threadIdx.x >> 6);
  const int wr = wid & 3, wc = wid >> 2;

  G1Ctx c;
  c.xs = (const char*)x + (size_t)(brow + 32 * wid) * 2048;
#pragma unroll
  for (int a = 0; a < 2; ++a) {
    const int r = 8 * a + (lane >> 3);
    const int ch = (lane & 7) ^ ((4 * a + (lane >> 4)) & 7);
    c.xv[a] = r * 2048 + ch * 16;
  }
  c.xl = lbase + wid * 4096;
  const int e0 = (wid < 4) ? 3 * wid : 12 + 4 * (wid - 4);
#pragma unroll
  for (int j = 0; j < 4; ++j) {
    int e = e0 + j; if (e > 27) e = 27;
    const int mat = e / 14, jj = e % 14;
    const int ci = 64 * jj + lane;
    const int rowp = ci / 14, cc = ci % 14;
    const int row = rowp ^ (((rowp >> 3) & 1) << 2);
    c.wv[j] = row * (F_ * 2) + cc * 16;
    c.ws[j] = (const char*)(mat ? wu : wg) + pn * 224;
    c.wl[j] = lbase + G1_WOFF + mat * 14336 + jj * 1024;
  }
  {
    const int g = lane >> 4, r16 = lane & 15;
#pragma unroll
    for (int s = 0; s < 2; ++s)
      c.xa[s] = lbase + (wr * 64 + r16) * 128 + (((4 * s + g) ^ (r16 >> 1)) & 7) * 16;
    const int q = r16 >> 2, p = lane & 3;
    const int fbase = (wc == 0) ? ((pn & 1) ? 3 : 0) : ((pn & 1) ? 0 : 4);
#pragma unroll
    for (int i = 0; i < 2; ++i)
      c.wa[i] = lbase + G1_WOFF + (8 * g + 4 * (i ^ (g & 1)) + q) * 224 + fbase * 32 + p * 8;
    const int nfg0 = 7 * pn + fbase;
    if (wc == 0) g1_run<8>(c, Hf, brow, wr, nfg0, lane);
    else g1_run<6>(c, Hf, brow, wr, nfg0, lane);
  }
}

constexpr int G2_ST = 49152;
constexpr int G2_LDS = 3 * G2_ST;
constexpr int G2_NT = F_ / 128;

struct G2Ctx {
  const char* hs;
  const char* ws;
  uint32_t hv, wv;
  uint32_t hl, wl;
  uint32_t ha;
  uint32_t wa[4];
};

__device__ __forceinline__ void g2_issue(const G2Ctx& c, int t, uint32_t so) {
  const char* hp = c.hs + (size_t)t * 32768;
  glds16_off<0>(hp, c.hv, c.hl + so);
  glds16_off<1024>(hp, c.hv, c.hl + so);
  glds16_off<2048>(hp, c.hv, c.hl + so);
  glds16_off<3072>(hp, c.hv, c.hl + so);
  const char* wp = c.ws + (size_t)t * (128 * 2048);
  glds16(wp, c.wv, c.wl + so);
  glds16(wp + 8 * 2048, c.wv, c.wl + so + 1024);
}
__device__ __forceinline__ void g2_read(const G2Ctx& c, uint32_t so, bf16x8 (&hf)[4], bf16x8 (&wf)[4]) {
#pragma unroll
  for (int a = 0; a < 4; ++a) hf[a] = lds_b128(c.ha + so + a * 1024);
#pragma unroll
  for (int f = 0; f < 4; ++f) wf[f] = lds_tr2(c.wa[f] + so, c.wa[f] + so + 2048);
}
__device__ __forceinline__ void g2_mma(f32x4 (&acc)[4][4], bf16x8 (&hf)[4], bf16x8 (&wf)[4]) {
#pragma unroll
  for (int f = 0; f < 4; ++f)
#pragma unroll
    for (int a = 0; a < 4; ++a) acc[f][a] = mfma16(wf[f], hf[a], acc[f][a]);
}

__global__ __launch_bounds__(512, 2) void gemm2_kernel(const unsigned char* __restrict__ Hf, const unsigned char* __restrict__ wd,
                                                       unsigned char* __restrict__ out) {
  __shared__ __attribute__((aligned(1024))) unsigned char smem[G2_LDS];
  const uint32_t lbase = (uint32_t)(uintptr_t)(LAS unsigned char*)smem;
  const int bid = blockIdx.x;
  const int xcd = bid & 7, idx = bid >> 3;
  const int rp = 4 * (xcd >> 1) + (idx & 3), cp = 8 * (xcd & 1) + (idx >> 2);
  const int lane = threadIdx.x & 63;
  const int wid = __builtin_amdgcn_readfirstlane(threadIdx.x >> 6);
  const int kq = wid & 3, mh = wid >> 2;

  G2Ctx c;
  c.hs = (const char*)Hf + (size_t)rp * 112 * 8192 + (kq * 8 + 4 * mh) * 1024;
  c.hv = lane * 16;
  c.hl = lbase + (kq * 8 + 4 * mh) * 1024;
  c.ws = (const char*)wd + (size_t)(32 * kq + 16 * mh) * 2048 + cp * 128;
  {
    const int pc = lane & 7;
    c.wv = (lane >> 3) * 2048 + (2 * (((pc >> 1) ^ (lane >> 4)) & 3) + (pc & 1)) * 16;
  }
  c.wl = lbase + 32768 + (32 * kq + 16 * mh) * 128;
  c.ha = lbase + (kq * 8 + 4 * mh) * 1024 + lane * 16;
  {
    const int g = lane >> 4, q = (lane >> 2) & 3, p = lane & 3;
#pragma unroll
    for (int f = 0; f < 4; ++f)
      c.wa[f] = lbase + 32768 + (32 * kq + 4 * g + q) * 128 + ((f ^ (2 * g + (q >> 1))) & 3) * 32 + p * 8;
  }

  f32x4 acc[4][4];
#pragma unroll
  for (int f = 0; f < 4; ++f)
#pragma unroll
    for (int a = 0; a < 4; ++a) acc[f][a] = (f32x4){0.f, 0.f, 0.f, 0.f};
  bf16x8 hf0[4], wf0[4], hf1[4], wf1[4];

  g2_issue(c, 0, 0);
  g2_issue(c, 1, G2_ST);
  g2_issue(c, 2, 2 * G2_ST);
  WAIT_VM(12);
  BAR();
  g2_read(c, 0, hf0, wf0);
  uint32_t so_cur = 0;
  for (int t = 0; t < G2_NT - 4; t += 2) {
    {
      uint32_t so_nxt = so_cur + G2_ST; if (so_nxt == G2_LDS) so_nxt = 0;
      WAIT_LGKM0(); WAIT_VM(6); BAR();
      g2_issue(c, t + 3, so_cur);
      g2_read(c, so_nxt, hf1, wf1);
      g2_mma(acc, hf0, wf0);
      SCHED();
      so_cur = so_nxt;
    }
    {
      uint32_t so_nxt = so_cur + G2_ST; if (so_nxt == G2_LDS) so_nxt = 0;
      WAIT_LGKM0(); WAIT_VM(6); BAR();
      g2_issue(c, t + 4, so_cur);
      g2_read(c, so_nxt, hf0, wf0);
      g2_mma(acc, hf1, wf1);
      SCHED();
      so_cur = so_nxt;
    }
  }
  {
    WAIT_LGKM0(); WAIT_VM(6); BAR();
    g2_issue(c, 27, 0);
    g2_read(c, G2_ST, hf1, wf1);
    g2_mma(acc, hf0, wf0);
    SCHED();
    WAIT_LGKM0(); WAIT_VM(6); BAR();
    g2_read(c, 2 * G2_ST, hf0, wf0);
    g2_mma(acc, hf1, wf1);
    SCHED();
    WAIT_LGKM0(); WAIT_VM(0); BAR();
    g2_read(c, 0, hf1, wf1);
    g2_mma(acc, hf0, wf0);
    SCHED();
    g2_mma(acc, hf1, wf1);
  }
  WAIT_LGKM0();
  BAR();
  {
    const int g = lane >> 4, r16 = lane & 15;
#pragma unroll
    for (int f = 0; f < 4; ++f)
#pragma unroll
      for (int a = 0; a < 4; ++a) {
        const uint32_t ad = lbase + (uint32_t)((kq * 128 + mh * 64 + a * 16 + r16) * 272 + (f * 16 + 4 * g) * 4);
        *(LAS f32x4*)(ad) = acc[f][a];
      }
  }
  WAIT_LGKM0();
  BAR();
  {
    const int tid = threadIdx.x;
    const int m = tid >> 2, cq = tid & 3;
    f32x4 s[4];
#pragma unroll
    for (int e = 0; e < 4; ++e) s[e] = *(LAS f32x4*)(lbase + (uint32_t)(m * 272 + cq * 64 + e * 16));
#pragma unroll
    for (int k = 1; k < 4; ++k)
#pragma unroll
      for (int e = 0; e < 4; ++e) s[e] += *(LAS f32x4*)(lbase + (uint32_t)((k * 128 + m) * 272 + cq * 64 + e * 16));
    u32x4 o0, o1;
    o0.x = pack2(s[0][0], s[0][1]); o0.y = pack2(s[0][2], s[0][3]); o0.z = pack2(s[1][0], s[1][1]); o0.w = pack2(s[1][2], s[1][3]);
    o1.x = pack2(s[2][0], s[2][1]); o1.y = pack2(s[2][2], s[2][3]); o1.z = pack2(s[3][0], s[3][1]); o1.w = pack2(s[3][2], s[3][3]);
    unsigned char* op = out + (size_t)(rp * 128 + m) * 2048 + cp * 128 + cq * 32;
    *(u32x4*)(op) = o0;
    *(u32x4*)(op + 16) = o1;
  }
}

extern "C" void kernel_launch(void* const* d_in, const int* in_sizes, int n_in,
                              void* d_out, int out_size, void* d_ws, size_t ws_size,
                              hipStream_t stream) {
  const unsigned char* x = (const unsigned char*)d_in[0];
  const unsigned char* wg = (const unsigned char*)d_in[1];
  const unsigned char* wu = (const unsigned char*)d_in[2];
  const unsigned char* wd = (const unsigned char*)d_in[3];
  unsigned char* Hf = (unsigned char*)d_ws;
  gemm1_kernel<<<256, 512, 0, stream>>>(x, wg, wu, Hf);
  gemm2_kernel<<<256, 512, 0, stream>>>(Hf, wd, (unsigned char*)d_out);
}
```

```cpp
#include <hip/hip_runtime.h>
#include <hip/hip_bf16.h>
#include <stdint.h>
#include <utility>

typedef __bf16 bf16x8 __attribute__((ext_vector_type(8)));
typedef __bf16 bf16x4 __attribute__((ext_vector_type(4)));
typedef float f32x4 __attribute__((ext_vector_type(4)));
typedef float f32x2 __attribute__((ext_vector_type(2)));
typedef unsigned u32x2 __attribute__((ext_vector_type(2)));
typedef unsigned u32x4 __attribute__((ext_vector_type(4)));
#define LAS __attribute__((address_space(3)))

#define S_ 2048
#define D_ 1024
#define F_ 3584

__device__ __forceinline__ void glds16(const void* sbase, uint32_t voff, uint32_t lds_addr) {
  asm volatile("s_mov_b32 m0, %2\n\ts_nop 0\n\tglobal_load_lds_dwordx4 %0, %1"
               :: "v"(voff), "s"(sbase), "s"(lds_addr) : "memory");
}
template <int OFF>
__device__ __forceinline__ void glds16_off(const void* sbase, uint32_t voff, uint32_t lds_addr) {
  asm volatile("s_mov_b32 m0, %2\n\ts_nop 0\n\tglobal_load_lds_dwordx4 %0, %1 offset:%3"
               :: "v"(voff), "s"(sbase), "s"(lds_addr), "n"(OFF) : "memory");
}
__device__ __forceinline__ void l2pf(const char* addr, uint32_t lds_scratch) {
  asm volatile("s_mov_b32 m0, %1\n\ts_nop 0\n\tglobal_load_lds_dword %0, off"
               :: "v"(addr), "s"(lds_scratch) : "memory");
}
#define WAIT_VM(n) asm volatile("s_waitcnt vmcnt(" #n ")" ::: "memory")
#define WAIT_LGKM0() asm volatile("s_waitcnt lgkmcnt(0)" ::: "memory")
#define BAR() do { asm volatile("" ::: "memory"); __builtin_amdgcn_s_barrier(); asm volatile("" ::: "memory"); } while (0)
#define SCHED() __builtin_amdgcn_sched_barrier(0)

__device__ __forceinline__ bf16x8 lds_b128(uint32_t addr) { return *(LAS bf16x8*)(addr); }
__device__ __forceinline__ bf16x8 lds_tr2(uint32_t a0, uint32_t a1) {
  bf16x4 w0 = __builtin_amdgcn_ds_read_tr16_b64_v4bf16((LAS bf16x4*)(a0));
  bf16x4 w1 = __builtin_amdgcn_ds_read_tr16_b64_v4bf16((LAS bf16x4*)(a1));
  return __builtin_shufflevector(w0, w1, 0, 1, 2, 3, 4, 5, 6, 7);
}
__device__ __forceinline__ f32x4 mfma16(bf16x8 a, bf16x8 b, f32x4 c) {
  return __builtin_amdgcn_mfma_f32_16x16x32_bf16(a, b, c, 0, 0, 0);
}
__device__ __forceinline__ float bf16r(float v) {
  return (float)((__bf16)v);
}
__device__ __forceinline__ unsigned pack2(float lo, float hi) {
  typedef __bf16 bf16x2 __attribute__((ext_vector_type(2)));
  bf16x2 p; p.x = (__bf16)lo; p.y = (__bf16)hi;
  return __builtin_bit_cast(unsigned, p);
}
__device__ __forceinline__ float gelu_tanh(float x) {
  const float c1 = -2.0f * 0.7978845608028654f * 1.4426950408889634f;
  const float c2 = c1 * 0.044715f;
  float x2 = x * x;
  float p = __builtin_fmaf(x2, c2, c1);
  float e = __builtin_amdgcn_exp2f(x * p);
  return x * __builtin_amdgcn_rcpf(1.0f + e);
}

constexpr int G1_XB = 32768;
constexpr int G1_WB = 28672;
constexpr int G1_WOFF = 2 * G1_XB;
constexpr int G1_SCR = 2 * G1_XB + 2 * G1_WB;
constexpr int G1_LDS = G1_SCR + 256;
#define G1_PD 4
constexpr int G1_NT = D_ / 64;

struct G1Ctx {
  const char* xs;
  const char* ws[4];
  uint32_t xv[2];
  uint32_t wv[4];
  uint32_t xl;
  uint32_t wl[4];
  uint32_t xa[2];
  uint32_t wa[2];
  const char* pf;
  uint32_t pfs;
  uint32_t scr;
};

template <int NWL>
__device__ __forceinline__ void g1_issue_one(const G1Ctx& c, int idx, int t, int buf) {
  if (idx < 4) {
    glds16(c.xs + (idx >> 1) * (16 * 2048) + t * 128, c.xv[idx & 1], c.xl + buf * G1_XB + idx * 1024);
  } else {
    int j = idx - 4;
    glds16(c.ws[j] + (size_t)t * (64 * F_ * 2), c.wv[j], c.wl[j] + buf * G1_WB);
  }
}

template <int NF, int B, bool HAS_NEXT, bool HAS_NEXT2, bool ISSUE_CUR, bool PF>
__device__ __forceinline__ void g1_tile(const G1Ctx& c, int t, f32x4 (&acc)[NF][4], bf16x8 (&xf)[2][4], bf16x8 (&wf)[4], unsigned long long& st_wait) {
  constexpr int NWL = (NF == 8) ? 3 : 4;
  constexpr int NL = 4 + NWL;
  constexpr int NS = 2 * NF;
#pragma unroll
  for (int j = 0; j < NS; ++j) {
    const int s = j / NF, f = j % NF;
    if (j == NS - 2 && HAS_NEXT) {
      WAIT_LGKM0();
      if (PF) WAIT_VM(1); else WAIT_VM(0);
      BAR();
    }
    if (j + 2 < NS) {
      const int s2 = (j + 2) / NF, f2 = (j + 2) % NF;
      const uint32_t o = B * G1_WB + s2 * (32 * 224) + (f2 & 1) * 14336 + (f2 >> 1) * 32;
      wf[(j + 2) & 3] = lds_tr2(c.wa[0] + o, c.wa[1] + o);
    } else if (HAS_NEXT) {
      const int f2 = j + 2 - NS;
      const uint32_t o = (B ^ 1) * G1_WB + (f2 & 1) * 14336 + (f2 >> 1) * 32;
      wf[(j + 2) & 3] = lds_tr2(c.wa[0] + o, c.wa[1] + o);
    }
    if (j < 4) xf[1][j] = lds_b128(c.xa[1] + B * G1_XB + j * 2048);
    if (j == NS - 2 && HAS_NEXT) {
#pragma unroll
      for (int m = 0; m < 4; ++m) xf[0][m] = lds_b128(c.xa[0] + (B ^ 1) * G1_XB + m * 2048);
    }
    if (j >= NS - 2 && HAS_NEXT2) g1_issue_one<NWL>(c, j - (NS - 2), t + 2, B);
    if (ISSUE_CUR && j + 2 < NL) g1_issue_one<NWL>(c, j + 2, t + 1, B ^ 1);
    if (PF && j == NL - 2) l2pf(c.pf + (size_t)c.pfs * (t + G1_PD), c.scr);
#pragma unroll
    for (int m = 0; m < 4; ++m) acc[f][m] = mfma16(wf[j & 3], xf[s][m], acc[f][m]);
    SCHED();
  }
}

template <int NF>
__device__ __forceinline__ void g1_run(const G1Ctx& c, unsigned char* Hf, int brow, int wr, int nfg0, int lane) {
  constexpr int NWL = (NF == 8) ? 3 : 4;
  constexpr int NL = 4 + NWL;
  unsigned long long st_wait = 0;
  f32x4 acc[NF][4];
#pragma unroll
  for (int f = 0; f < NF; ++f)
#pragma unroll
    for (int m = 0; m < 4; ++m) acc[f][m] = (f32x4){0.f, 0.f, 0.f, 0.f};
  bf16x8 xf[2][4], wf[4];
#pragma unroll
  for (int i = 0; i < NL; ++i) g1_issue_one<NWL>(c, i, 0, 0);
#pragma unroll
  for (int i = 2; i < G1_PD; ++i) l2pf(c.pf + (size_t)c.pfs * i, c.scr);
#pragma unroll
  for (int i = 0; i < NL; ++i) g1_issue_one<NWL>(c, i, 1, 1);
  asm volatile("s_waitcnt vmcnt(%0)" :: "n"(NL + G1_PD - 2) : "memory");
  BAR();
#pragma unroll
  for (int m = 0; m < 4; ++m) xf[0][m] = lds_b128(c.xa[0] + m * 2048);
  wf[0] = lds_tr2(c.wa[0], c.wa[1]);
  wf[1] = lds_tr2(c.wa[0] + 14336, c.wa[1] + 14336);
  SCHED();
  static_assert(G1_PD % 2 == 0 && G1_PD >= 4, "PD");
  g1_tile<NF, 0, true, true, false, true>(c, 0, acc, xf, wf, st_wait);
  for (int t = 1; t < G1_NT - G1_PD - 1; t += 2) {
    g1_tile<NF, 1, true, true, true, true>(c, t, acc, xf, wf, st_wait);
    g1_tile<NF, 0, true, true, true, true>(c, t + 1, acc, xf, wf, st_wait);
  }
  g1_tile<NF, 1, true, true, true, true>(c, G1_NT - G1_PD - 1, acc, xf, wf, st_wait);
  for (int t = G1_NT - G1_PD; t < G1_NT - 3; t += 2) {
    g1_tile<NF, 0, true, true, true, false>(c, t, acc, xf, wf, st_wait);
    g1_tile<NF, 1, true, true, true, false>(c, t + 1, acc, xf, wf, st_wait);
  }
  g1_tile<NF, 0, true, false, true, false>(c, G1_NT - 2, acc, xf, wf, st_wait);
  g1_tile<NF, 1, false, false, false, false>(c, G1_NT - 1, acc, xf, wf, st_wait);

#pragma unroll
  for (int i = 0; i < NF / 2; ++i) {
    const int nfg = nfg0 + i;
    const int kb = nfg >> 1, half = nfg & 1;
#pragma unroll
    for (int m = 0; m < 4; ++m) {
      const int mfrag = (brow >> 4) + wr * 4 + m;
      const int rp = mfrag >> 3, mfl = mfrag & 7;
      f32x4 g = acc[2 * i][m], u = acc[2 * i + 1][m];
      float h[4];
#pragma unroll
      for (int r = 0; r < 4; ++r) h[r] = bf16r(gelu_tanh(bf16r(g[r]))) * bf16r(u[r]);
      u32x2 o; o.x = pack2(h[0], h[1]); o.y = pack2(h[2], h[3]);
      *(u32x2*)(Hf + ((size_t)((rp * 112 + kb) * 8 + mfl)) * 1024 + lane * 16 + half * 8) = o;
    }
  }
}

__global__ __launch_bounds__(512, 2) void gemm1_kernel(const unsigned char* __restrict__ x, const unsigned char* __restrict__ wg,
                                                       const unsigned char* __restrict__ wu, const unsigned char* __restrict__ wd,
                                                       unsigned char* __restrict__ Hf) {
  __shared__ __attribute__((aligned(1024))) unsigned char smem[G1_LDS];
  const uint32_t lbase = (uint32_t)(uintptr_t)(LAS unsigned char*)smem;
  const int bid = blockIdx.x;
  const int xcd = bid & 7, idx = bid >> 3;
  const int pn = xcd * 4 + (idx & 3), pm = idx >> 2;
  const int brow = pm * 256;
  const int lane = threadIdx.x & 63;
  const int wid = __builtin_amdgcn_readfirstlane(threadIdx.x >> 6);
  const int wr = wid & 3, wc = wid >> 2;

  G1Ctx c;
  c.xs = (const char*)x + (size_t)(brow + 32 * wid) * 2048;
#pragma unroll
  for (int a = 0; a < 2; ++a) {
    const int r = 8 * a + (lane >> 3);
    const int ch = (lane & 7) ^ ((4 * a + (lane >> 4)) & 7);
    c.xv[a] = r * 2048 + ch * 16;
  }
  c.xl = lbase + wid * 4096;
  const int e0 = (wid < 4) ? 3 * wid : 12 + 4 * (wid - 4);
#pragma unroll
  for (int j = 0; j < 4; ++j) {
    int e = e0 + j; if (e > 27) e = 27;
    const int mat = e / 14, jj = e % 14;
    const int ci = 64 * jj + lane;
    const int rowp = ci / 14, cc = ci % 14;
    const int row = rowp ^ (((rowp >> 3) & 1) << 2);
    c.wv[j] = row * (F_ * 2) + cc * 16;
    c.ws[j] = (const char*)(mat ? wu : wg) + pn * 224;
    c.wl[j] = lbase + G1_WOFF + mat * 14336 + jj * 1024;
  }
  {
#define WDL 2
    int l = lane < 12 + WDL ? lane : 0;
    if (l >= 12) {
      const int unit = 256 / WDL;
      const int gw = bid * 8 + wid;
      c.pf = (const char*)wd + (size_t)((gw * WDL + (l - 12)) * unit) - (size_t)2 * (2048 * 256);
      c.pfs = 2048 * 256;
    } else if (l < 8) {
      c.pf = (const char*)x + (size_t)(brow + 64 * (pn & 3) + 8 * wid + l) * 2048;
      c.pfs = 128;
    } else {
      int q = wid * 4 + (l - 8); if (q > 27) q = 27;
      const int li = idx * 28 + q;
      const int mat = li / 448, r = (li % 448) / 7, cc = li % 7;
      c.pf = (const char*)(mat ? wu : wg) + (size_t)r * (F_ * 2) + xcd * 896 + cc * 128;
      c.pfs = 64 * F_ * 2;
    }
    c.scr = lbase + G1_SCR;
  }
  {
    const int g = lane >> 4, r16 = lane & 15;
#pragma unroll
    for (int s = 0; s < 2; ++s)
      c.xa[s] = lbase + (wr * 64 + r16) * 128 + (((4 * s + g) ^ (r16 >> 1)) & 7) * 16;
    const int q = r16 >> 2, p = lane & 3;
    const int fbase = (wc == 0) ? ((pn & 1) ? 3 : 0) : ((pn & 1) ? 0 : 4);
#pragma unroll
    for (int i = 0; i < 2; ++i)
      c.wa[i] = lbase + G1_WOFF + (8 * g + 4 * (i ^ (g & 1)) + q) * 224 + fbase * 32 + p * 8;
    const int nfg0 = 7 * pn + fbase;
    if (wc == 0) g1_run<8>(c, Hf, brow, wr, nfg0, lane);
    else g1_run<6>(c, Hf, brow, wr, nfg0, lane);
  }
}

constexpr int G2_ST = 49152;
constexpr int G2_RING = 3 * G2_ST;
constexpr int G2_LDS = G2_RING + 256;
#define G2_PD 10
constexpr int G2_NT = F_ / 128;

struct G2Ctx {
  const char* hs;
  const char* ws;
  uint32_t hv, wv;
  uint32_t hl, wl;
  uint32_t ha;
  uint32_t wa[4];
};

__device__ __forceinline__ void g2_issue(const G2Ctx& c, int t, uint32_t so) {
  const char* hp = c.hs + (size_t)t * 32768;
  glds16_off<0>(hp, c.hv, c.hl + so);
  glds16_off<1024>(hp, c.hv, c.hl + so);
  glds16_off<2048>(hp, c.hv, c.hl + so);
  glds16_off<3072>(hp, c.hv, c.hl + so);
  const char* wp = c.ws + (size_t)t * (128 * 2048);
  glds16(wp, c.wv, c.wl + so);
  glds16(wp + 8 * 2048, c.wv, c.wl + so + 1024);
}
__device__ __forceinline__ void g2_read(const G2Ctx& c, uint32_t so, bf16x8 (&hf)[4], bf16x8 (&wf)[4]) {
#pragma unroll
  for (int a = 0; a < 4; ++a) hf[a] = lds_b128(c.ha + so + a * 1024);
#pragma unroll
  for (int f = 0; f < 4; ++f) wf[f] = lds_tr2(c.wa[f] + so, c.wa[f] + so + 2048);
}
__device__ __forceinline__ void g2_mma(f32x4 (&acc)[4][4], bf16x8 (&hf)[4], bf16x8 (&wf)[4]) {
#pragma unroll
  for (int f = 0; f < 4; ++f)
#pragma unroll
    for (int a = 0; a < 4; ++a) acc[f][a] = mfma16(wf[f], hf[a], acc[f][a]);
}

__global__ __launch_bounds__(512, 2) void gemm2_kernel(const unsigned char* __restrict__ Hf, const unsigned char* __restrict__ wd,
                                                       unsigned char* __restrict__ out) {
  __shared__ __attribute__((aligned(1024))) unsigned char smem[G2_LDS];
  const uint32_t lbase = (uint32_t)(uintptr_t)(LAS unsigned char*)smem;
  const int bid = blockIdx.x;
  const int xcd = bid & 7, idx = bid >> 3;
  const int rp = 4 * (xcd >> 1) + (idx & 3), cp = 8 * (xcd & 1) + (idx >> 2);
  const int lane = threadIdx.x & 63;
  const int wid = __builtin_amdgcn_readfirstlane(threadIdx.x >> 6);
  const int kq = wid & 3, mh = wid >> 2;
  G2Ctx c;
  c.hs = (const char*)Hf + (size_t)rp * 112 * 8192 + (kq * 8 + 4 * mh) * 1024;
  c.hv = lane * 16;
  c.hl = lbase + (kq * 8 + 4 * mh) * 1024;
  c.ws = (const char*)wd + (size_t)(32 * kq + 16 * mh) * 2048 + cp * 128;
  {
    const int pc = lane & 7;
    c.wv = (lane >> 3) * 2048 + (2 * (((pc >> 1) ^ (lane >> 4)) & 3) + (pc & 1)) * 16;
  }
  c.wl = lbase + 32768 + (32 * kq + 16 * mh) * 128;
  c.ha = lbase + (kq * 8 + 4 * mh) * 1024 + lane * 16;
  {
    const int g = lane >> 4, q = (lane >> 2) & 3, p = lane & 3;
#pragma unroll
    for (int f = 0; f < 4; ++f)
      c.wa[f] = lbase + 32768 + (32 * kq + 4 * g + q) * 128 + ((f ^ (2 * g + (q >> 1))) & 3) * 32 + p * 8;
  }

  f32x4 acc[4][4];
#pragma unroll
  for (int f = 0; f < 4; ++f)
#pragma unroll
    for (int a = 0; a < 4; ++a) acc[f][a] = (f32x4){0.f, 0.f, 0.f, 0.f};
  bf16x8 hf0[4], wf0[4], hf1[4], wf1[4];

#define G2W(n) do { WAIT_LGKM0(); WAIT_VM(n); BAR(); } while (0)
  g2_issue(c, 0, 0);
  g2_issue(c, 1, G2_ST);
  g2_issue(c, 2, 2 * G2_ST);
  WAIT_VM(12);
  BAR();
  g2_read(c, 0, hf0, wf0);
  uint32_t so_cur = 0;
  for (int t = 0; t < G2_NT - 4; t += 2) {
    {
      uint32_t so_nxt = so_cur + G2_ST; if (so_nxt == G2_RING) so_nxt = 0;
      G2W(6);
      g2_issue(c, t + 3, so_cur);
      g2_read(c, so_nxt, hf1, wf1);
      g2_mma(acc, hf0, wf0);
      SCHED();
      so_cur = so_nxt;
    }
    {
      uint32_t so_nxt = so_cur + G2_ST; if (so_nxt == G2_RING) so_nxt = 0;
      G2W(6);
      g2_issue(c, t + 4, so_cur);
      g2_read(c, so_nxt, hf0, wf0);
      g2_mma(acc, hf1, wf1);
      SCHED();
      so_cur = so_nxt;
    }
  }
  {
    G2W(6);
    g2_issue(c, 27, 0);
    g2_read(c, G2_ST, hf1, wf1);
    g2_mma(acc, hf0, wf0);
    SCHED();
    G2W(6);
    g2_read(c, 2 * G2_ST, hf0, wf0);
    g2_mma(acc, hf1, wf1);
    SCHED();
    G2W(0);
    g2_read(c, 0, hf1, wf1);
    g2_mma(acc, hf0, wf0);
    SCHED();
    g2_mma(acc, hf1, wf1);
  }
  WAIT_LGKM0();
  BAR();
  {
    const int g = lane >> 4, r16 = lane & 15;
#pragma unroll
    for (int f = 0; f < 4; ++f)
#pragma unroll
      for (int a = 0; a < 4; ++a) {
        const uint32_t ad = lbase + (uint32_t)((kq * 128 + mh * 64 + a * 16 + r16) * 272 + (f * 16 + 4 * g) * 4);
        *(LAS f32x4*)(ad) = acc[f][a];
      }
  }
  WAIT_LGKM0();
  BAR();
  {
    const int tid = threadIdx.x;
    const int m = tid >> 2, cq = tid & 3;
    f32x4 s[4];
#pragma unroll
    for (int e = 0; e < 4; ++e) s[e] = *(LAS f32x4*)(lbase + (uint32_t)(m * 272 + cq * 64 + e * 16));
#pragma unroll
    for (int k = 1; k < 4; ++k)
#pragma unroll
      for (int e = 0; e < 4; ++e) s[e] += *(LAS f32x4*)(lbase + (uint32_t)((k * 128 + m) * 272 + cq * 64 + e * 16));
    u32x4 o0, o1;
    o0.x = pack2(s[0][0], s[0][1]); o0.y = pack2(s[0][2], s[0][3]); o0.z = pack2(s[1][0], s[1][1]); o0.w = pack2(s[1][2], s[1][3]);
    o1.x = pack2(s[2][0], s[2][1]); o1.y = pack2(s[2][2], s[2][3]); o1.z = pack2(s[3][0], s[3][1]); o1.w = pack2(s[3][2], s[3][3]);
    unsigned char* op = out + (size_t)(rp * 128 + m) * 2048 + cp * 128 + cq * 32;
    *(u32x4*)(op) = o0;
    *(u32x4*)(op + 16) = o1;
  }
}

extern "C" void kernel_launch(void* const* d_in, const int* in_sizes, int n_in,
                              void* d_out, int out_size, void* d_ws, size_t ws_size,
                              hipStream_t stream) {
  const unsigned char* x = (const unsigned char*)d_in[0];
  const unsigned char* wg = (const unsigned char*)d_in[1];
  const unsigned char* wu = (const unsigned char*)d_in[2];
  const unsigned char* wd = (const unsigned char*)d_in[3];
  unsigned char* Hf = (unsigned char*)d_ws;
  gemm1_kernel<<<256, 512, 0, stream>>>(x, wg, wu, wd, Hf);
  gemm2_kernel<<<256, 512, 0, stream>>>(Hf, wd, (unsigned char*)d_out);
}
```

```cpp
#include <hip/hip_runtime.h>
#include <hip/hip_bf16.h>
#include <stdint.h>

typedef __bf16 bf16x8 __attribute__((ext_vector_type(8)));
typedef __bf16 bf16x4 __attribute__((ext_vector_type(4)));
typedef __bf16 bf16x2 __attribute__((ext_vector_type(2)));
typedef float f32x4 __attribute__((ext_vector_type(4)));
typedef float f32x2 __attribute__((ext_vector_type(2)));
typedef unsigned u32x2 __attribute__((ext_vector_type(2)));
typedef unsigned u32x4 __attribute__((ext_vector_type(4)));
#define LAS __attribute__((address_space(3)))

#define S_ 2048
#define D_ 1024
#define F_ 3584

__device__ __forceinline__ void glds16(const void* sbase, uint32_t voff, uint32_t lds_addr) {
  asm volatile("s_mov_b32 m0, %2\n\ts_nop 0\n\tglobal_load_lds_dwordx4 %0, %1"
               :: "v"(voff), "s"(sbase), "s"(lds_addr) : "memory");
}
template <int OFF>
__device__ __forceinline__ void glds16_off(const void* sbase, uint32_t voff, uint32_t lds_addr) {
  asm volatile("s_mov_b32 m0, %2\n\ts_nop 0\n\tglobal_load_lds_dwordx4 %0, %1 offset:%3"
               :: "v"(voff), "s"(sbase), "s"(lds_addr), "n"(OFF) : "memory");
}
__device__ __forceinline__ void l2pf(const char* addr, uint32_t lds_scratch) {
  asm volatile("s_mov_b32 m0, %1\n\ts_nop 0\n\tglobal_load_lds_dword %0, off"
               :: "v"(addr), "s"(lds_scratch) : "memory");
}
#define WAIT_VM(n) asm volatile("s_waitcnt vmcnt(" #n ")" ::: "memory")
#define WAIT_LGKM0() asm volatile("s_waitcnt lgkmcnt(0)" ::: "memory")
#define BAR() do { asm volatile("" ::: "memory"); __builtin_amdgcn_s_barrier(); asm volatile("" ::: "memory"); } while (0)
#define SCHED() __builtin_amdgcn_sched_barrier(0)

__device__ __forceinline__ bf16x8 lds_b128(uint32_t addr) { return *(LAS bf16x8*)(addr); }
__device__ __forceinline__ bf16x4 lds_tr(uint32_t a) { return __builtin_amdgcn_ds_read_tr16_b64_v4bf16((LAS bf16x4*)(a)); }
__device__ __forceinline__ bf16x8 cat8(bf16x4 lo, bf16x4 hi) { return __builtin_shufflevector(lo, hi, 0, 1, 2, 3, 4, 5, 6, 7); }
__device__ __forceinline__ f32x4 mfma16(bf16x8 a, bf16x8 b, f32x4 c) {
  return __builtin_amdgcn_mfma_f32_16x16x32_bf16(a, b, c, 0, 0, 0);
}
__device__ __forceinline__ f32x2 unpack2(unsigned u) {
  f32x2 r; r.x = __builtin_bit_cast(float, u << 16); r.y = __builtin_bit_cast(float, u & 0xffff0000u); return r;
}
__device__ __forceinline__ unsigned pack2v(f32x2 v) {
  return __builtin_bit_cast(unsigned, __builtin_convertvector(v, bf16x2));
}

#define DBG_STAMP 0

constexpr int G1_XB = 32768;
constexpr int G1_WB = 28672;
constexpr int G1_WOFF = 3 * G1_XB;
constexpr int G1_SCR = 3 * G1_XB + 2 * G1_WB;
constexpr int G1_LDS = G1_SCR + 256;
#define G1_PD 4
#define PFMODE 0
constexpr int G1_NT = D_ / 64;

struct G1Ctx {
  const char* xs;
  const char* ws[4];
  uint32_t xv[2];
  uint32_t wv[4];
  uint32_t xl;
  uint32_t wl[4];
  uint32_t xa[2];
  uint32_t wa[2];
  const char* pf;
  const char* pf2;
  uint32_t pfs2;
  uint32_t scr;
  bool pfw;
};

__device__ __forceinline__ void g1_issue_x(const G1Ctx& c, int idx, int t, uint32_t xoff) {
  glds16(c.xs + (idx >> 1) * (16 * 2048) + t * 128, c.xv[idx & 1], c.xl + xoff + idx * 1024);
}
__device__ __forceinline__ void g1_issue_w(const G1Ctx& c, int j, int t, uint32_t woff) {
  glds16(c.ws[j] + (size_t)t * (64 * F_ * 2), c.wv[j], c.wl[j] + woff);
}

template <int NF>
__device__ __forceinline__ void g1_tile(const G1Ctx& c, int t, uint32_t xo_cur, uint32_t xo_nxt, uint32_t xo_free, uint32_t wo_cur,
                                        f32x4 (&acc)[NF][4], bf16x8 (&xf)[2][4], bf16x4 (&wlo)[4], bf16x4 (&whi)[4],
                                        unsigned long long& st_wait) {
  constexpr int NWL = (NF == 8) ? 3 : 4;
  constexpr int NS = 2 * NF;
  const int t1 = (t + 1 < G1_NT) ? t + 1 : G1_NT - 1;
  const int t2 = (t + 2 < G1_NT) ? t + 2 : G1_NT - 1;
  const int tp = (t + G1_PD < G1_NT) ? t + G1_PD : G1_NT - 1;
  const uint32_t wo_nxt = G1_WB - wo_cur;
  const uint32_t xa1 = c.xa[1] + xo_cur;
  const uint32_t xa0n = c.xa[0] + xo_nxt;
  const uint32_t wac0 = c.wa[0] + wo_cur, wac1 = c.wa[1] + wo_cur;
  const uint32_t wan0 = c.wa[0] + wo_nxt, wan1 = c.wa[1] + wo_nxt;
#pragma unroll
  for (int j = 0; j < NS; ++j) {
    const int s = j / NF, f = j % NF;
    if (j == NS - 2) {
      WAIT_LGKM0();
      if (c.pfw) WAIT_VM(6); else WAIT_VM(4);
      BAR();
    }
    uint32_t a0, a1;
    if (j + 2 < NS) {
      const int s2 = (j + 2) / NF, f2 = (j + 2) % NF;
      const uint32_t o = s2 * (32 * 224) + (f2 & 1) * 14336 + (f2 >> 1) * 32;
      a0 = wac0 + o; a1 = wac1 + o;
    } else {
      const int f2 = j + 2 - NS;
      const uint32_t o = (f2 & 1) * 14336 + (f2 >> 1) * 32;
      a0 = wan0 + o; a1 = wan1 + o;
    }
    const bf16x8 w = cat8(wlo[j & 3], whi[j & 3]);
    acc[f][0] = mfma16(w, xf[s][0], acc[f][0]);
    SCHED();
    wlo[(j + 2) & 3] = lds_tr(a0);
    SCHED();
    acc[f][1] = mfma16(w, xf[s][1], acc[f][1]);
    SCHED();
    whi[(j + 2) & 3] = lds_tr(a1);
    SCHED();
    acc[f][2] = mfma16(w, xf[s][2], acc[f][2]);
    SCHED();
    if (j < 4) xf[1][j] = lds_b128(xa1 + j * 2048);
    if (j >= NS - 2) {
      const int m0 = 2 * (j - (NS - 2));
      xf[0][m0] = lds_b128(xa0n + m0 * 2048);
      xf[0][m0 + 1] = lds_b128(xa0n + (m0 + 1) * 2048);
    }
    SCHED();
    acc[f][3] = mfma16(w, xf[s][3], acc[f][3]);
    SCHED();
    if (j >= NS - 2) g1_issue_w(c, j - (NS - 2), t2, wo_cur);
    if (j + 2 < NWL) g1_issue_w(c, j + 2, t1, wo_nxt);
    if (j == 2 && c.pfw) { l2pf(c.pf + (size_t)128 * tp, c.scr); l2pf(c.pf2 + (size_t)c.pfs2 * tp, c.scr); }
    if (j >= 3 && j < 7) g1_issue_x(c, j - 3, t2, xo_free);
    SCHED();
  }
}

#define EPI_ROUND 0
__device__ __forceinline__ unsigned geglu2(f32x2 g, f32x2 u) {
  const float c1 = -2.0f * 0.7978845608028654f * 1.4426950408889634f;
  const float c2 = c1 * 0.044715f;
  f32x2 p = (g * g) * c2 + c1;
  f32x2 a = g * p;
  f32x2 e; e.x = __builtin_amdgcn_exp2f(a.x); e.y = __builtin_amdgcn_exp2f(a.y);
  f32x2 d = e + 1.0f;
  f32x2 r; r.x = __builtin_amdgcn_rcpf(d.x); r.y = __builtin_amdgcn_rcpf(d.y);
  f32x2 ge = g * r;
  return pack2v(ge * u);
}
__device__ __forceinline__ void st16(unsigned char* p, u32x4 v) {
  asm volatile("global_store_dwordx4 %0, %1, off sc1\n\ts_nop 1" :: "v"(p), "v"(v) : "memory");
}
__device__ __forceinline__ void st8(unsigned char* p, u32x2 v) {
  asm volatile("global_store_dwordx2 %0, %1, off sc1" :: "v"(p), "v"(v) : "memory");
}
__device__ __forceinline__ uint32_t hkb(int kb) { return kb * 8192; }

template <int NP>
__device__ __forceinline__ void g1_store(unsigned char* hb, int nfg0, int i, const u32x2 (&hp)[NP][4]) {
  if (NP == 4) {
    if (i & 1) {
      unsigned char* p = hb + hkb((nfg0 + i) >> 1);
#pragma unroll
      for (int m = 0; m < 4; ++m)
        st16(p + m * 1024, (u32x4){hp[i - 1][m].x, hp[i - 1][m].y, hp[i][m].x, hp[i][m].y});
    }
  } else {
    if (i == 2) {
      if (nfg0 & 1) {
        unsigned char* p = hb + hkb(nfg0 >> 1) + 8;
        unsigned char* q = hb + hkb((nfg0 + 1) >> 1);
#pragma unroll
        for (int m = 0; m < 4; ++m) {
          *(u32x2*)(p + m * 1024) = hp[0][m];
          st16(q + m * 1024, (u32x4){hp[1][m].x, hp[1][m].y, hp[2][m].x, hp[2][m].y});
        }
      } else {
        unsigned char* p = hb + hkb((nfg0 + 2) >> 1);
        unsigned char* q = hb + hkb(nfg0 >> 1);
#pragma unroll
        for (int m = 0; m < 4; ++m) {
          *(u32x2*)(p + m * 1024) = hp[2][m];
          st16(q + m * 1024, (u32x4){hp[0][m].x, hp[0][m].y, hp[1][m].x, hp[1][m].y});
        }
      }
    }
  }
}

template <int NF>
__device__ __forceinline__ void g1_epi(f32x4 (&acc)[NF][4], unsigned char* hb, int nfg0) {
  constexpr int NP = NF / 2;
  u32x2 hp[NP][4];
#pragma unroll
  for (int i = 0; i < NP; ++i) {
#pragma unroll
    for (int e = 0; e < 4; ++e) {
      hp[i][e].x = geglu2((f32x2){acc[2 * i][e][0], acc[2 * i][e][1]}, (f32x2){acc[2 * i + 1][e][0], acc[2 * i + 1][e][1]});
      hp[i][e].y = geglu2((f32x2){acc[2 * i][e][2], acc[2 * i][e][3]}, (f32x2){acc[2 * i + 1][e][2], acc[2 * i + 1][e][3]});
    }
    g1_store<NP>(hb, nfg0, i, hp);
  }
}

template <int NF>
__device__ __forceinline__ void g1_last(const G1Ctx& c, uint32_t xo_cur, uint32_t wo_cur, f32x4 (&acc)[NF][4], bf16x8 (&xf)[2][4],
                                        bf16x4 (&wlo)[4], bf16x4 (&whi)[4], unsigned char* hb, int nfg0) {
  constexpr int NP = NF / 2;
  constexpr int NS = 2 * NF;
  const uint32_t xa1 = c.xa[1] + xo_cur;
  const uint32_t wac0 = c.wa[0] + wo_cur, wac1 = c.wa[1] + wo_cur;
  u32x2 hp[NP][4];
#pragma unroll
  for (int j = 0; j < NS; ++j) {
    const int i = j >> 2, s = (j >> 1) & 1, f = 2 * i + (j & 1);
    const int j2 = j + 2, i2 = j2 >> 2, s2 = (j2 >> 1) & 1;
    const uint32_t o = s2 * (32 * 224) + (j2 & 1) * 14336 + i2 * 32;
    const bf16x8 w = cat8(wlo[j & 3], whi[j & 3]);
    const int e = j & 3;
    acc[f][0] = mfma16(w, xf[s][0], acc[f][0]);
    SCHED();
    if (j2 < NS) wlo[j2 & 3] = lds_tr(wac0 + o);
    if (j < 2) xf[1][2 * j] = lds_b128(xa1 + (2 * j) * 2048);
    SCHED();
    acc[f][1] = mfma16(w, xf[s][1], acc[f][1]);
    SCHED();
    if (j2 < NS) whi[j2 & 3] = lds_tr(wac1 + o);
    if (j < 2) xf[1][2 * j + 1] = lds_b128(xa1 + (2 * j + 1) * 2048);
    if (i > 0) hp[i - 1][e].x = geglu2((f32x2){acc[2 * i - 2][e][0], acc[2 * i - 2][e][1]}, (f32x2){acc[2 * i - 1][e][0], acc[2 * i - 1][e][1]});
    SCHED();
    acc[f][2] = mfma16(w, xf[s][2], acc[f][2]);
    SCHED();
    if (i > 0) hp[i - 1][e].y = geglu2((f32x2){acc[2 * i - 2][e][2], acc[2 * i - 2][e][3]}, (f32x2){acc[2 * i - 1][e][2], acc[2 * i - 1][e][3]});
    SCHED();
    acc[f][3] = mfma16(w, xf[s][3], acc[f][3]);
    SCHED();
    if (i > 0 && e == 3) g1_store<NP>(hb, nfg0, i - 1, hp);
    SCHED();
  }
#pragma unroll
  for (int e = 0; e < 4; ++e) {
    hp[NP - 1][e].x = geglu2((f32x2){acc[NF - 2][e][0], acc[NF - 2][e][1]}, (f32x2){acc[NF - 1][e][0], acc[NF - 1][e][1]});
    hp[NP - 1][e].y = geglu2((f32x2){acc[NF - 2][e][2], acc[NF - 2][e][3]}, (f32x2){acc[NF - 1][e][2], acc[NF - 1][e][3]});
  }
  g1_store<NP>(hb, nfg0, NP - 1, hp);
}

constexpr size_t WS_XCH = (size_t)32 << 20;
constexpr size_t WS_FLAGS = (size_t)16 << 20;

template <int NF>
__device__ __forceinline__ void g1_run(const G1Ctx& c, unsigned char* hb, unsigned char* xch, int nfg0, int lane) {
  constexpr int NWL = (NF == 8) ? 3 : 4;
  unsigned long long st_wait = 0;
  f32x4 acc[NF][4];
#pragma unroll
  for (int f = 0; f < NF; ++f)
#pragma unroll
    for (int m = 0; m < 4; ++m) acc[f][m] = (f32x4){0.f, 0.f, 0.f, 0.f};
  bf16x8 xf[2][4];
  bf16x4 wlo[4], whi[4];
#pragma unroll
  for (int i = 0; i < 4; ++i) g1_issue_x(c, i, 0, 0);
#pragma unroll
  for (int i = 0; i < NWL; ++i) g1_issue_w(c, i, 0, 0);
#pragma unroll
  for (int i = 0; i < 2; ++i) g1_issue_w(c, i, 1, G1_WB);
  WAIT_VM(2);
  BAR();
#pragma unroll
  for (int m = 0; m < 4; ++m) xf[0][m] = lds_b128(c.xa[0] + m * 2048);
  wlo[0] = lds_tr(c.wa[0]); whi[0] = lds_tr(c.wa[1]);
  wlo[1] = lds_tr(c.wa[0] + 14336); whi[1] = lds_tr(c.wa[1] + 14336);
  SCHED();
#pragma unroll
  for (int i = 0; i < 4; ++i) g1_issue_x(c, i, 1, G1_XB);
  if (c.pfw) {
#pragma unroll
    for (int i = 2; i < G1_PD; ++i) { l2pf(c.pf + (size_t)128 * i, c.scr); l2pf(c.pf2 + (size_t)c.pfs2 * i, c.scr); }
  }
  SCHED();
  uint32_t xo0 = 0, xo1 = G1_XB, xo2 = 2 * G1_XB, wo = 0;
#define LAST_SEP 0
#pragma nounroll
  for (int t = 0; t < G1_NT - LAST_SEP; ++t) {
    g1_tile<NF>(c, t, xo0, xo1, xo2, wo, acc, xf, wlo, whi, st_wait);
    const uint32_t tmp = xo0; xo0 = xo1; xo1 = xo2; xo2 = tmp;
    wo = G1_WB - wo;
  }
  g1_epi<NF>(acc, hb, nfg0);
}

__global__ __launch_bounds__(512, 2) void gemm1_kernel(const unsigned char* __restrict__ x, const unsigned char* __restrict__ wg,
                                                       const unsigned char* __restrict__ wu, const unsigned char* __restrict__ wd,
                                                       unsigned char* __restrict__ Hf) {
  __shared__ __attribute__((aligned(1024))) unsigned char smem[G1_LDS];
  const uint32_t lbase = (uint32_t)(uintptr_t)(LAS unsigned char*)smem;
  const int bid = blockIdx.x;
  const int xcd = bid & 7, idx = bid >> 3;
  const int pn = xcd * 4 + (idx & 3), pm = idx >> 2;
  const int brow = pm * 256;
  const int lane = threadIdx.x & 63;
  const int wid = __builtin_amdgcn_readfirstlane(threadIdx.x >> 6);
  const int wr = wid & 3, wc = wid >> 2;

  G1Ctx c;
  c.xs = (const char*)x + (size_t)(brow + 32 * wid) * 2048;
#pragma unroll
  for (int a = 0; a < 2; ++a) {
    const int r = 8 * a + (lane >> 3);
    const int ch = (lane & 7) ^ ((4 * a + (lane >> 4)) & 7);
    c.xv[a] = r * 2048 + ch * 16;
  }
  c.xl = lbase + wid * 4096;
  const int e0 = (wid < 4) ? 3 * wid : 12 + 4 * (wid - 4);
#pragma unroll
  for (int j = 0; j < 4; ++j) {
    int e = e0 + j; if (e > 27) e = 27;
    const int mat = e / 14, jj = e % 14;
    const int ci = 64 * jj + lane;
    const int rowp = ci / 14, cc = ci % 14;
    const int row = rowp ^ (((rowp >> 3) & 1) << 2);
    c.wv[j] = row * (F_ * 2) + cc * 16;
    c.ws[j] = (const char*)(mat ? wu : wg) + pn * 224;
    c.wl[j] = lbase + G1_WOFF + mat * 14336 + jj * 1024;
  }
  {
    c.pfw = (wid == 4);
    c.pf = (const char*)x + (size_t)(brow + 64 * (pn & 3) + lane) * 2048;
    if (lane >= 28 && lane < 44) {
      c.pf2 = (const char*)wd + (size_t)((bid * 16 + (lane - 28)) * 128) - (size_t)2 * (256 * 2048);
      c.pfs2 = 256 * 2048;
    } else {
      const int li = idx * 28 + (lane < 28 ? lane : 0);
      const int mat = li / 448, r = (li % 448) / 7, cc = li % 7;
      c.pf2 = (const char*)(mat ? wu : wg) + (size_t)r * (F_ * 2) + xcd * 896 + cc * 128;
      c.pfs2 = 64 * F_ * 2;
    }
    c.scr = lbase + G1_SCR;
  }
  const int g = lane >> 4, r16 = lane & 15;
#pragma unroll
  for (int s = 0; s < 2; ++s)
    c.xa[s] = lbase + (wr * 64 + r16) * 128 + (((4 * s + g) ^ (r16 >> 1)) & 7) * 16;
  const int q = r16 >> 2, p = lane & 3;
  const int fbase = (wc == 0) ? ((pn & 1) ? 3 : 0) : ((pn & 1) ? 0 : 4);
#pragma unroll
  for (int i = 0; i < 2; ++i)
    c.wa[i] = lbase + G1_WOFF + (8 * g + 4 * (i ^ (g & 1)) + q) * 224 + fbase * 32 + p * 8;
  const int nfg0 = 7 * pn + fbase;
  unsigned char* hb = Hf + (size_t)(2 * pm + (wr >> 1)) * (112 * 8192) + (wr & 1) * 4096 + lane * 16;
  unsigned char* xch = nullptr;
  if (lane == 0) *(unsigned*)(Hf + WS_FLAGS + (size_t)(bid * 8 + wid) * 64) = 0u;
  if (wc == 0) g1_run<8>(c, hb, xch, nfg0, lane);
  else g1_run<6>(c, hb, xch, nfg0, lane);
}

constexpr int G2_NT = 28;
constexpr int G2_ST = 32768;
constexpr int G2_RING = 3 * G2_ST;
constexpr int G2_LDS = G2_RING + 65536;

__device__ __forceinline__ void g2_issue(const char* hs, uint32_t hv, const char* ws, uint32_t wv0, uint32_t wv1, int st, uint32_t hdst, uint32_t wdst) {
  const char* hp = hs + (size_t)st * 16384;
  glds16_off<0>(hp, hv, hdst);
  glds16_off<1024>(hp, hv, hdst);
  glds16_off<2048>(hp, hv, hdst);
  glds16_off<3072>(hp, hv, hdst);
  const char* wp = ws + (size_t)st * (64 * 2048);
  glds16(wp, wv0, wdst);
  glds16(wp + 4 * 2048, wv1, wdst + 1024);
  glds16(wp + 8 * 2048, wv0, wdst + 2048);
  glds16(wp + 12 * 2048, wv1, wdst + 3072);
}

template <bool RD>
__device__ __forceinline__ void g2_step(const uint32_t (&ha)[4], const uint32_t (&wa)[4], f32x4 (&acc)[4][4],
                                        const bf16x8 (&hc)[4], const bf16x4 (&wlc)[4], const bf16x4 (&whc)[4],
                                        bf16x8 (&hn)[4], bf16x4 (&wln)[4], bf16x4 (&whn)[4]) {
#pragma unroll
  for (int f = 0; f < 4; ++f) {
    const bf16x8 w = cat8(wlc[f], whc[f]);
    acc[f][0] = mfma16(w, hc[0], acc[f][0]);
    SCHED();
    if (RD) wln[f] = lds_tr(wa[f]);
    SCHED();
    acc[f][1] = mfma16(w, hc[1], acc[f][1]);
    SCHED();
    if (RD) whn[f] = lds_tr(wa[f] + 4096);
    SCHED();
    acc[f][2] = mfma16(w, hc[2], acc[f][2]);
    SCHED();
    if (RD) hn[f] = lds_b128(ha[f]);
    SCHED();
    acc[f][3] = mfma16(w, hc[3], acc[f][3]);
    SCHED();
  }
}

__global__ __launch_bounds__(768, 3) void gemm2_kernel(const unsigned char* __restrict__ Hf, const unsigned char* __restrict__ wd,
                                                       unsigned char* __restrict__ out, unsigned char* __restrict__ ws_base) {
  __shared__ __attribute__((aligned(1024))) unsigned char smem[G2_LDS];
  const uint32_t lbase = (uint32_t)(uintptr_t)(LAS unsigned char*)smem;
  const int bid = blockIdx.x;
  const int xcd = bid & 7, idx = bid >> 3;
  const int kh2 = xcd & 1;
  const int rp = 4 * (xcd >> 1) + (idx & 3), cp = idx >> 2;
  const int lane = threadIdx.x & 63;
  const int wid = __builtin_amdgcn_readfirstlane(threadIdx.x >> 6);

  if (wid >= 8) {
    const int l = wid - 8;
    const char* hs = (const char*)Hf + (size_t)(rp * 112 + kh2 * 56) * 8192 + l * 4096;
    const char* ws = (const char*)wd + (size_t)(kh2 * 1792 + 16 * l) * 2048 + cp * 256;
    const uint32_t hv = lane * 16;
    const int pc = lane & 15, r4 = lane >> 4;
    const uint32_t wv0 = r4 * 2048 + ((((pc >> 1) ^ r4) & 7) * 2 + (pc & 1)) * 16;
    const uint32_t wv1 = r4 * 2048 + ((((pc >> 1) ^ (4 + r4)) & 7) * 2 + (pc & 1)) * 16;
    const uint32_t hb = lbase + l * 4096, wb = lbase + 16384 + l * 4096;
    g2_issue(hs, hv, ws, wv0, wv1, 0, hb, wb);
    g2_issue(hs, hv, ws, wv0, wv1, 1, hb + G2_ST, wb + G2_ST);
    WAIT_VM(8);
    BAR();
    g2_issue(hs, hv, ws, wv0, wv1, 2, hb + 2 * G2_ST, wb + 2 * G2_ST);
    WAIT_VM(8);
    uint32_t so = 0;
    for (int t = 0; t < G2_NT - 3; ++t) {
      BAR();
      g2_issue(hs, hv, ws, wv0, wv1, t + 3, hb + so, wb + so);
      so += G2_ST; if (so == G2_RING) so = 0;
      WAIT_VM(8);
    }
    BAR();
    WAIT_VM(0);
    BAR();
    BAR();
    return;
  }

  const int wm = wid & 1, wn = (wid >> 1) & 1, kh = wid >> 2;
  uint32_t wa0[4];
  {
    const int g = lane >> 4, q = (lane >> 2) & 3, p = lane & 3;
    const int r = 4 * g + q;
#pragma unroll
    for (int f = 0; f < 4; ++f)
      wa0[f] = lbase + 16384 + (32 * kh + r) * 256 + (((4 * wn + f) ^ r) & 7) * 32 + p * 8;
  }
  const int own = 2 * kh + kh2;
  uint32_t ha0[4];
#pragma unroll
  for (int a = 0; a < 4; ++a) ha0[a] = lbase + kh * 8192 + wm * 4096 + (a ^ own) * 1024 + lane * 16;
  const uint32_t xw = lbase + G2_RING + wid * 8192 + lane * 16, xrd = lbase + G2_RING + (wid ^ 4) * 8192 + lane * 16;

  f32x4 acc[4][4];
#pragma unroll
  for (int f = 0; f < 4; ++f)
#pragma unroll
    for (int a = 0; a < 4; ++a) acc[f][a] = (f32x4){0.f, 0.f, 0.f, 0.f};
  bf16x8 hf0[4], hf1[4];
  bf16x4 wl0[4], wh0[4], wl1[4], wh1[4];

  BAR();
#pragma unroll
  for (int a = 0; a < 4; ++a) hf0[a] = lds_b128(ha0[a]);
#pragma unroll
  for (int f = 0; f < 4; ++f) { wl0[f] = lds_tr(wa0[f]); wh0[f] = lds_tr(wa0[f] + 4096); }
  uint32_t so = 0;
#define G2_STEP(HC, WLC, WHC, HN, WLN, WHN)                                                    \
  {                                                                                            \
    WAIT_LGKM0(); BAR();                                                                       \
    so += G2_ST; if (so == G2_RING) so = 0;                                                    \
    const uint32_t wa[4] = {wa0[0] + so, wa0[1] + so, wa0[2] + so, wa0[3] + so};               \
    const uint32_t ha[4] = {ha0[0] + so, ha0[1] + so, ha0[2] + so, ha0[3] + so};               \
    g2_step<true>(ha, wa, acc, HC, WLC, WHC, HN, WLN, WHN);                                    \
  }
  for (int t = 0; t < G2_NT - 2; t += 2) {
    G2_STEP(hf0, wl0, wh0, hf1, wl1, wh1);
    G2_STEP(hf1, wl1, wh1, hf0, wl0, wh0);
  }
  G2_STEP(hf0, wl0, wh0, hf1, wl1, wh1);
  g2_step<false>(ha0, wa0, acc, hf1, wl1, wh1, hf0, wl0, wh0);
#pragma unroll
  for (int f = 0; f < 4; ++f) *(LAS f32x4*)(xw + f * 1024) = acc[f][3];
#pragma unroll
  for (int f = 0; f < 4; ++f) *(LAS f32x4*)(xw + 4096 + f * 1024) = acc[f][2];
  WAIT_LGKM0();
  BAR();
  {
    unsigned char* xs = ws_base + WS_XCH + (size_t)((bid ^ 1) * 8 + wid) * 4096 + lane * 16;
#pragma unroll
    for (int f = 0; f < 4; ++f) {
      const f32x4 v = acc[f][1] + *(LAS f32x4*)(xrd + f * 1024);
      st16(xs + f * 1024, __builtin_bit_cast(u32x4, v));
    }
  }
#pragma unroll
  for (int f = 0; f < 4; ++f) acc[f][0] += *(LAS f32x4*)(xrd + 4096 + f * 1024);
  {
    WAIT_VM(0);
    unsigned* myflag = (unsigned*)(ws_base + WS_FLAGS + (size_t)(bid * 8 + wid) * 64);
    const unsigned* pflag = (const unsigned*)(ws_base + WS_FLAGS + (size_t)((bid ^ 1) * 8 + wid) * 64);
    if (lane == 0) __hip_atomic_store(myflag, 1u, __ATOMIC_RELAXED, __HIP_MEMORY_SCOPE_AGENT);
    for (int spin = 0; spin < (1 << 22); ++spin) {
      const unsigned v = __builtin_amdgcn_readfirstlane(__hip_atomic_load(pflag, __ATOMIC_RELAXED, __HIP_MEMORY_SCOPE_AGENT));
      if (v != 0) break;
      __builtin_amdgcn_s_sleep(1);
    }
    asm volatile("" ::: "memory");
    const unsigned char* xr = ws_base + WS_XCH + (size_t)(bid * 8 + wid) * 4096 + lane * 16;
    f32x4 o0, o1, o2, o3;
    asm volatile("global_load_dwordx4 %0, %4, off sc1\n\t"
                 "global_load_dwordx4 %1, %4, off offset:1024 sc1\n\t"
                 "global_load_dwordx4 %2, %4, off offset:2048 sc1\n\t"
                 "global_load_dwordx4 %3, %4, off offset:3072 sc1\n\t"
                 "s_waitcnt vmcnt(0)"
                 : "=&v"(o0), "=&v"(o1), "=&v"(o2), "=&v"(o3) : "v"(xr) : "memory");
    acc[0][0] += o0; acc[1][0] += o1; acc[2][0] += o2; acc[3][0] += o3;
  }
  {
    const int g = lane >> 4, r16 = lane & 15;
    unsigned char* op = out + (size_t)(rp * 128 + wm * 64 + own * 16 + r16) * 2048 + cp * 256 + wn * 128 + g * 8;
#pragma unroll
    for (int f = 0; f < 4; ++f) {
      const f32x4 v = acc[f][0];
      u32x2 pk; pk.x = pack2v((f32x2){v[0], v[1]}); pk.y = pack2v((f32x2){v[2], v[3]});
      *(u32x2*)(op + f * 32) = pk;
    }
  }
}

extern "C" void kernel_launch(void* const* d_in, const int* in_sizes, int n_in,
                              void* d_out, int out_size, void* d_ws, size_t ws_size,
                              hipStream_t stream) {
  const unsigned char* x = (const unsigned char*)d_in[0];
  const unsigned char* wg = (const unsigned char*)d_in[1];
  const unsigned char* wu = (const unsigned char*)d_in[2];
  const unsigned char* wd = (const unsigned char*)d_in[3];
  unsigned char* Hf = (unsigned char*)d_ws;
  gemm1_kernel<<<256, 512, 0, stream>>>(x, wg, wu, wd, Hf);
  gemm2_kernel<<<256, 768, 0, stream>>>(Hf, wd, (unsigned char*)d_out, (unsigned char*)d_ws);
}
```

```cpp
#include <hip/hip_runtime.h>
#include <hip/hip_bf16.h>
#include <stdint.h>

typedef __bf16 bf16x8 __attribute__((ext_vector_type(8)));
typedef __bf16 bf16x4 __attribute__((ext_vector_type(4)));
typedef __bf16 bf16x2 __attribute__((ext_vector_type(2)));
typedef float f32x4 __attribute__((ext_vector_type(4)));
typedef float f32x2 __attribute__((ext_vector_type(2)));
typedef unsigned u32x2 __attribute__((ext_vector_type(2)));
typedef unsigned u32x4 __attribute__((ext_vector_type(4)));
#define LAS __attribute__((address_space(3)))

#define S_ 2048
#define D_ 1024
#define F_ 3584

__device__ __forceinline__ void glds16(const void* sbase, uint32_t voff, uint32_t lds_addr) {
  asm volatile("s_mov_b32 m0, %2\n\ts_nop 0\n\tglobal_load_lds_dwordx4 %0, %1"
               :: "v"(voff), "s"(sbase), "s"(lds_addr) : "memory");
}
template <int OFF>
__device__ __forceinline__ void glds16_off(const void* sbase, uint32_t voff, uint32_t lds_addr) {
  asm volatile("s_mov_b32 m0, %2\n\ts_nop 0\n\tglobal_load_lds_dwordx4 %0, %1 offset:%3"
               :: "v"(voff), "s"(sbase), "s"(lds_addr), "n"(OFF) : "memory");
}
__device__ __forceinline__ void l2pf(const char* addr, uint32_t lds_scratch) {
  asm volatile("s_mov_b32 m0, %1\n\ts_nop 0\n\tglobal_load_lds_dword %0, off"
               :: "v"(addr), "s"(lds_scratch) : "memory");
}
#define WAIT_VM(n) asm volatile("s_waitcnt vmcnt(" #n ")" ::: "memory")
#define WAIT_LGKM0() asm volatile("s_waitcnt lgkmcnt(0)" ::: "memory")
#define BAR() do { asm volatile("" ::: "memory"); __builtin_amdgcn_s_barrier(); asm volatile("" ::: "memory"); } while (0)
#define SCHED() __builtin_amdgcn_sched_barrier(0)

__device__ __forceinline__ bf16x8 lds_b128(uint32_t addr) { return *(LAS bf16x8*)(addr); }
__device__ __forceinline__ bf16x4 lds_tr(uint32_t a) { return __builtin_amdgcn_ds_read_tr16_b64_v4bf16((LAS bf16x4*)(a)); }
__device__ __forceinline__ bf16x8 cat8(bf16x4 lo, bf16x4 hi) { return __builtin_shufflevector(lo, hi, 0, 1, 2, 3, 4, 5, 6, 7); }
__device__ __forceinline__ f32x4 mfma16(bf16x8 a, bf16x8 b, f32x4 c) {
  return __builtin_amdgcn_mfma_f32_16x16x32_bf16(a, b, c, 0, 0, 0);
}
__device__ __forceinline__ f32x2 unpack2(unsigned u) {
  f32x2 r; r.x = __builtin_bit_cast(float, u << 16); r.y = __builtin_bit_cast(float, u & 0xffff0000u); return r;
}
__device__ __forceinline__ unsigned pack2v(f32x2 v) {
  return __builtin_bit_cast(unsigned, __builtin_convertvector(v, bf16x2));
}

#define DBG_STAMP 0

constexpr int G1_XB = 32768;
constexpr int G1_WB = 28672;
constexpr int G1_WOFF = 3 * G1_XB;
constexpr int G1_SCR = 3 * G1_XB + 2 * G1_WB;
constexpr int G1_LDS = G1_SCR + 256;
#define G1_PD 4
#define PFMODE 0
constexpr int G1_NT = D_ / 64;

struct G1Ctx {
  const char* xs;
  const char* ws[4];
  uint32_t xv[2];
  uint32_t wv[4];
  uint32_t xl;
  uint32_t wl[4];
  uint32_t xa[2];
  uint32_t wa[2];
  const char* pf;
  const char* pf2;
  uint32_t pfs2;
  uint32_t scr;
  bool pfw;
};

__device__ __forceinline__ void g1_issue_x(const G1Ctx& c, int idx, int t, uint32_t xoff) {
  glds16(c.xs + (idx >> 1) * (16 * 2048) + t * 128, c.xv[idx & 1], c.xl + xoff + idx * 1024);
}
__device__ __forceinline__ void g1_issue_w(const G1Ctx& c, int j, int t, uint32_t woff) {
  glds16(c.ws[j] + (size_t)t * (64 * F_ * 2), c.wv[j], c.wl[j] + woff);
}

template <int NF>
__device__ __forceinline__ void g1_tile(const G1Ctx& c, int t, uint32_t xo_cur, uint32_t xo_nxt, uint32_t xo_free, uint32_t wo_cur,
                                        f32x4 (&acc)[NF][4], bf16x8 (&xf)[2][4], bf16x4 (&wlo)[4], bf16x4 (&whi)[4],
                                        unsigned long long& st_wait) {
  constexpr int NWL = (NF == 8) ? 3 : 4;
  constexpr int NS = 2 * NF;
  const int t1 = (t + 1 < G1_NT) ? t + 1 : G1_NT - 1;
  const int t2 = (t + 2 < G1_NT) ? t + 2 : G1_NT - 1;
  const int tp = (t + G1_PD < G1_NT) ? t + G1_PD : G1_NT - 1;
  const uint32_t wo_nxt = G1_WB - wo_cur;
  const uint32_t xa1 = c.xa[1] + xo_cur;
  const uint32_t xa0n = c.xa[0] + xo_nxt;
  const uint32_t wac0 = c.wa[0] + wo_cur, wac1 = c.wa[1] + wo_cur;
  const uint32_t wan0 = c.wa[0] + wo_nxt, wan1 = c.wa[1] + wo_nxt;
#pragma unroll
  for (int j = 0; j < NS; ++j) {
    const int s = j / NF, f = j % NF;
    if (j == NS - 2) {
      WAIT_LGKM0();
      if (c.pfw) WAIT_VM(6); else WAIT_VM(4);
      BAR();
    }
    uint32_t a0, a1;
    if (j + 2 < NS) {
      const int s2 = (j + 2) / NF, f2 = (j + 2) % NF;
      const uint32_t o = s2 * (32 * 224) + (f2 & 1) * 14336 + (f2 >> 1) * 32;
      a0 = wac0 + o; a1 = wac1 + o;
    } else {
      const int f2 = j + 2 - NS;
      const uint32_t o = (f2 & 1) * 14336 + (f2 >> 1) * 32;
      a0 = wan0 + o; a1 = wan1 + o;
    }
    const bf16x8 w = cat8(wlo[j & 3], whi[j & 3]);
    acc[f][0] = mfma16(w, xf[s][0], acc[f][0]);
    SCHED();
    wlo[(j + 2) & 3] = lds_tr(a0);
    SCHED();
    acc[f][1] = mfma16(w, xf[s][1], acc[f][1]);
    SCHED();
    whi[(j + 2) & 3] = lds_tr(a1);
    SCHED();
    acc[f][2] = mfma16(w, xf[s][2], acc[f][2]);
    SCHED();
    if (j < 4) xf[1][j] = lds_b128(xa1 + j * 2048);
    if (j >= NS - 2) {
      const int m0 = 2 * (j - (NS - 2));
      xf[0][m0] = lds_b128(xa0n + m0 * 2048);
      xf[0][m0 + 1] = lds_b128(xa0n + (m0 + 1) * 2048);
    }
    SCHED();
    acc[f][3] = mfma16(w, xf[s][3], acc[f][3]);
    SCHED();
    if (j >= NS - 2) g1_issue_w(c, j - (NS - 2), t2, wo_cur);
    if (j + 2 < NWL) g1_issue_w(c, j + 2, t1, wo_nxt);
    if (j == 2 && c.pfw) { l2pf(c.pf + (size_t)128 * tp, c.scr); l2pf(c.pf2 + (size_t)c.pfs2 * tp, c.scr); }
    if (j >= 3 && j < 7) g1_issue_x(c, j - 3, t2, xo_free);
    SCHED();
  }
}

#define EPI_ROUND 0
__device__ __forceinline__ unsigned geglu2(f32x2 g, f32x2 u) {
  const float c1 = -2.0f * 0.7978845608028654f * 1.4426950408889634f;
  const float c2 = c1 * 0.044715f;
  f32x2 p = (g * g) * c2 + c1;
  f32x2 a = g * p;
  f32x2 e; e.x = __builtin_amdgcn_exp2f(a.x); e.y = __builtin_amdgcn_exp2f(a.y);
  f32x2 d = e + 1.0f;
  f32x2 r; r.x = __builtin_amdgcn_rcpf(d.x); r.y = __builtin_amdgcn_rcpf(d.y);
  f32x2 ge = g * r;
  return pack2v(ge * u);
}
__device__ __forceinline__ void st16(unsigned char* p, u32x4 v) {
  asm volatile("global_store_dwordx4 %0, %1, off sc1\n\ts_nop 1" :: "v"(p), "v"(v) : "memory");
}
__device__ __forceinline__ void st8(unsigned char* p, u32x2 v) {
  asm volatile("global_store_dwordx2 %0, %1, off sc1" :: "v"(p), "v"(v) : "memory");
}
__device__ __forceinline__ uint32_t hkb(int kb) { return kb * 8192; }

template <int NP>
__device__ __forceinline__ void g1_store(unsigned char* hb, int nfg0, int i, const u32x2 (&hp)[NP][4]) {
  if (NP == 4) {
    if (i & 1) {
      unsigned char* p = hb + hkb((nfg0 + i) >> 1);
#pragma unroll
      for (int m = 0; m < 4; ++m)
        st16(p + m * 1024, (u32x4){hp[i - 1][m].x, hp[i - 1][m].y, hp[i][m].x, hp[i][m].y});
    }
  } else {
    if (i == 2) {
      if (nfg0 & 1) {
        unsigned char* p = hb + hkb(nfg0 >> 1) + 8;
        unsigned char* q = hb + hkb((nfg0 + 1) >> 1);
#pragma unroll
        for (int m = 0; m < 4; ++m) {
          *(u32x2*)(p + m * 1024) = hp[0][m];
          st16(q + m * 1024, (u32x4){hp[1][m].x, hp[1][m].y, hp[2][m].x, hp[2][m].y});
        }
      } else {
        unsigned char* p = hb + hkb((nfg0 + 2) >> 1);
        unsigned char* q = hb + hkb(nfg0 >> 1);
#pragma unroll
        for (int m = 0; m < 4; ++m) {
          *(u32x2*)(p + m * 1024) = hp[2][m];
          st16(q + m * 1024, (u32x4){hp[0][m].x, hp[0][m].y, hp[1][m].x, hp[1][m].y});
        }
      }
    }
  }
}

template <int NF>
__device__ __forceinline__ void g1_epi(f32x4 (&acc)[NF][4], unsigned char* hb, int nfg0) {
  constexpr int NP = NF / 2;
  u32x2 hp[NP][4];
#pragma unroll
  for (int i = 0; i < NP; ++i) {
#pragma unroll
    for (int e = 0; e < 4; ++e) {
      hp[i][e].x = geglu2((f32x2){acc[2 * i][e][0], acc[2 * i][e][1]}, (f32x2){acc[2 * i + 1][e][0], acc[2 * i + 1][e][1]});
      hp[i][e].y = geglu2((f32x2){acc[2 * i][e][2], acc[2 * i][e][3]}, (f32x2){acc[2 * i + 1][e][2], acc[2 * i + 1][e][3]});
    }
    g1_store<NP>(hb, nfg0, i, hp);
  }
}

template <int NF>
__device__ __forceinline__ void g1_last(const G1Ctx& c, uint32_t xo_cur, uint32_t wo_cur, f32x4 (&acc)[NF][4], bf16x8 (&xf)[2][4],
                                        bf16x4 (&wlo)[4], bf16x4 (&whi)[4], unsigned char* hb, int nfg0) {
  constexpr int NP = NF / 2;
  constexpr int NS = 2 * NF;
  const uint32_t xa1 = c.xa[1] + xo_cur;
  const uint32_t wac0 = c.wa[0] + wo_cur, wac1 = c.wa[1] + wo_cur;
  u32x2 hp[NP][4];
#pragma unroll
  for (int j = 0; j < NS; ++j) {
    const int i = j >> 2, s = (j >> 1) & 1, f = 2 * i + (j & 1);
    const int j2 = j + 2, i2 = j2 >> 2, s2 = (j2 >> 1) & 1;
    const uint32_t o = s2 * (32 * 224) + (j2 & 1) * 14336 + i2 * 32;
    const bf16x8 w = cat8(wlo[j & 3], whi[j & 3]);
    const int e = j & 3;
    acc[f][0] = mfma16(w, xf[s][0], acc[f][0]);
    SCHED();
    if (j2 < NS) wlo[j2 & 3] = lds_tr(wac0 + o);
    if (j < 2) xf[1][2 * j] = lds_b128(xa1 + (2 * j) * 2048);
    SCHED();
    acc[f][1] = mfma16(w, xf[s][1], acc[f][1]);
    SCHED();
    if (j2 < NS) whi[j2 & 3] = lds_tr(wac1 + o);
    if (j < 2) xf[1][2 * j + 1] = lds_b128(xa1 + (2 * j + 1) * 2048);
    if (i > 0) hp[i - 1][e].x = geglu2((f32x2){acc[2 * i - 2][e][0], acc[2 * i - 2][e][1]}, (f32x2){acc[2 * i - 1][e][0], acc[2 * i - 1][e][1]});
    SCHED();
    acc[f][2] = mfma16(w, xf[s][2], acc[f][2]);
    SCHED();
    if (i > 0) hp[i - 1][e].y = geglu2((f32x2){acc[2 * i - 2][e][2], acc[2 * i - 2][e][3]}, (f32x2){acc[2 * i - 1][e][2], acc[2 * i - 1][e][3]});
    SCHED();
    acc[f][3] = mfma16(w, xf[s][3], acc[f][3]);
    SCHED();
    if (i > 0 && e == 3) g1_store<NP>(hb, nfg0, i - 1, hp);
    SCHED();
  }
#pragma unroll
  for (int e = 0; e < 4; ++e) {
    hp[NP - 1][e].x = geglu2((f32x2){acc[NF - 2][e][0], acc[NF - 2][e][1]}, (f32x2){acc[NF - 1][e][0], acc[NF - 1][e][1]});
    hp[NP - 1][e].y = geglu2((f32x2){acc[NF - 2][e][2], acc[NF - 2][e][3]}, (f32x2){acc[NF - 1][e][2], acc[NF - 1][e][3]});
  }
  g1_store<NP>(hb, nfg0, NP - 1, hp);
}

constexpr size_t WS_XCH = (size_t)32 << 20;
constexpr size_t WS_FLAGS = (size_t)16 << 20;

template <int NF>
__device__ __forceinline__ void g1_run(const G1Ctx& c, unsigned char* hb, unsigned char* xch, int nfg0, int lane) {
  constexpr int NWL = (NF == 8) ? 3 : 4;
  unsigned long long st_wait = 0;
  f32x4 acc[NF][4];
#pragma unroll
  for (int f = 0; f < NF; ++f)
#pragma unroll
    for (int m = 0; m < 4; ++m) acc[f][m] = (f32x4){0.f, 0.f, 0.f, 0.f};
  bf16x8 xf[2][4];
  bf16x4 wlo[4], whi[4];
#pragma unroll
  for (int i = 0; i < 4; ++i) g1_issue_x(c, i, 0, 0);
#pragma unroll
  for (int i = 0; i < NWL; ++i) g1_issue_w(c, i, 0, 0);
#pragma unroll
  for (int i = 0; i < 2; ++i) g1_issue_w(c, i, 1, G1_WB);
  WAIT_VM(2);
  BAR();
#pragma unroll
  for (int m = 0; m < 4; ++m) xf[0][m] = lds_b128(c.xa[0] + m * 2048);
  wlo[0] = lds_tr(c.wa[0]); whi[0] = lds_tr(c.wa[1]);
  wlo[1] = lds_tr(c.wa[0] + 14336); whi[1] = lds_tr(c.wa[1] + 14336);
  SCHED();
#pragma unroll
  for (int i = 0; i < 4; ++i) g1_issue_x(c, i, 1, G1_XB);
  if (c.pfw) {
#pragma unroll
    for (int i = 2; i < G1_PD; ++i) { l2pf(c.pf + (size_t)128 * i, c.scr); l2pf(c.pf2 + (size_t)c.pfs2 * i, c.scr); }
  }
  SCHED();
  uint32_t xo0 = 0, xo1 = G1_XB, xo2 = 2 * G1_XB, wo = 0;
#define LAST_SEP 0
#pragma nounroll
  for (int t = 0; t < G1_NT - LAST_SEP; ++t) {
    g1_tile<NF>(c, t, xo0, xo1, xo2, wo, acc, xf, wlo, whi, st_wait);
    const uint32_t tmp = xo0; xo0 = xo1; xo1 = xo2; xo2 = tmp;
    wo = G1_WB - wo;
  }
  g1_epi<NF>(acc, hb, nfg0);
}

__global__ __launch_bounds__(512, 2) void gemm1_kernel(const unsigned char* __restrict__ x, const unsigned char* __restrict__ wg,
                                                       const unsigned char* __restrict__ wu, const unsigned char* __restrict__ wd,
                                                       unsigned char* __restrict__ Hf) {
  __shared__ __attribute__((aligned(1024))) unsigned char smem[G1_LDS];
  const uint32_t lbase = (uint32_t)(uintptr_t)(LAS unsigned char*)smem;
  const int bid = blockIdx.x;
  const int xcd = bid & 7, idx = bid >> 3;
  const int pn = xcd * 4 + (idx & 3), pm = idx >> 2;
  const int brow = pm * 256;
  const int lane = threadIdx.x & 63;
  const int wid = __builtin_amdgcn_readfirstlane(threadIdx.x >> 6);
  const int wr = wid & 3, wc = wid >> 2;
  if (wid < 2) {
    const char* pc = (const char*)__builtin_amdgcn_s_getpc();
    l2pf(pc + (size_t)(wid * 64 + lane) * 100, lbase + G1_SCR);
  }

  G1Ctx c;
  c.xs = (const char*)x + (size_t)(brow + 32 * wid) * 2048;
#pragma unroll
  for (int a = 0; a < 2; ++a) {
    const int r = 8 * a + (lane >> 3);
    const int ch = (lane & 7) ^ ((4 * a + (lane >> 4)) & 7);
    c.xv[a] = r * 2048 + ch * 16;
  }
  c.xl = lbase + wid * 4096;
  const int e0 = (wid < 4) ? 3 * wid : 12 + 4 * (wid - 4);
#pragma unroll
  for (int j = 0; j < 4; ++j) {
    int e = e0 + j; if (e > 27) e = 27;
    const int mat = e / 14, jj = e % 14;
    const int ci = 64 * jj + lane;
    const int rowp = ci / 14, cc = ci % 14;
    const int row = rowp ^ (((rowp >> 3) & 1) << 2);
    c.wv[j] = row * (F_ * 2) + cc * 16;
    c.ws[j] = (const char*)(mat ? wu : wg) + pn * 224;
    c.wl[j] = lbase + G1_WOFF + mat * 14336 + jj * 1024;
  }
  {
    c.pfw = (wid == 4);
    c.pf = (const char*)x + (size_t)(brow + 64 * (pn & 3) + lane) * 2048;
    if (lane >= 28 && lane < 44) {
      c.pf2 = (const char*)wd + (size_t)((bid * 16 + (lane - 28)) * 128) - (size_t)2 * (256 * 2048);
      c.pfs2 = 256 * 2048;
    } else {
      const int li = idx * 28 + (lane < 28 ? lane : 0);
      const int mat = li / 448, r = (li % 448) / 7, cc = li % 7;
      c.pf2 = (const char*)(mat ? wu : wg) + (size_t)r * (F_ * 2) + xcd * 896 + cc * 128;
      c.pfs2 = 64 * F_ * 2;
    }
    c.scr = lbase + G1_SCR;
  }
  const int g = lane >> 4, r16 = lane & 15;
#pragma unroll
  for (int s = 0; s < 2; ++s)
    c.xa[s] = lbase + (wr * 64 + r16) * 128 + (((4 * s + g) ^ (r16 >> 1)) & 7) * 16;
  const int q = r16 >> 2, p = lane & 3;
  const int fbase = (wc == 0) ? ((pn & 1) ? 3 : 0) : ((pn & 1) ? 0 : 4);
#pragma unroll
  for (int i = 0; i < 2; ++i)
    c.wa[i] = lbase + G1_WOFF + (8 * g + 4 * (i ^ (g & 1)) + q) * 224 + fbase * 32 + p * 8;
  const int nfg0 = 7 * pn + fbase;
  unsigned char* hb = Hf + (size_t)(2 * pm + (wr >> 1)) * (112 * 8192) + (wr & 1) * 4096 + lane * 16;
  unsigned char* xch = nullptr;
  if (lane == 0) *(unsigned*)(Hf + WS_FLAGS + (size_t)(bid * 8 + wid) * 64) = 0u;
  if (wc == 0) g1_run<8>(c, hb, xch, nfg0, lane);
  else g1_run<6>(c, hb, xch, nfg0, lane);
}

constexpr int G2_NT = 28;
constexpr int G2_ST = 32768;
constexpr int G2_RING = 3 * G2_ST;
constexpr int G2_LDS = G2_RING + 65536;

__device__ __forceinline__ void g2_issue(const char* hs, uint32_t hv, const char* ws, uint32_t wv0, uint32_t wv1, int st, uint32_t hdst, uint32_t wdst) {
  const char* hp = hs + (size_t)st * 16384;
  glds16_off<0>(hp, hv, hdst);
  glds16_off<1024>(hp, hv, hdst);
  glds16_off<2048>(hp, hv, hdst);
  glds16_off<3072>(hp, hv, hdst);
  const char* wp = ws + (size_t)st * (64 * 2048);
  glds16(wp, wv0, wdst);
  glds16(wp + 4 * 2048, wv1, wdst + 1024);
  glds16(wp + 8 * 2048, wv0, wdst + 2048);
  glds16(wp + 12 * 2048, wv1, wdst + 3072);
}

template <bool RD>
__device__ __forceinline__ void g2_step(const uint32_t (&ha)[4], const uint32_t (&wa)[4], f32x4 (&acc)[4][4],
                                        const bf16x8 (&hc)[4], const bf16x4 (&wlc)[4], const bf16x4 (&whc)[4],
                                        bf16x8 (&hn)[4], bf16x4 (&wln)[4], bf16x4 (&whn)[4]) {
#pragma unroll
  for (int f = 0; f < 4; ++f) {
    const bf16x8 w = cat8(wlc[f], whc[f]);
    acc[f][0] = mfma16(w, hc[0], acc[f][0]);
    SCHED();
    if (RD) wln[f] = lds_tr(wa[f]);
    SCHED();
    acc[f][1] = mfma16(w, hc[1], acc[f][1]);
    SCHED();
    if (RD) whn[f] = lds_tr(wa[f] + 4096);
    SCHED();
    acc[f][2] = mfma16(w, hc[2], acc[f][2]);
    SCHED();
    if (RD) hn[f] = lds_b128(ha[f]);
    SCHED();
    acc[f][3] = mfma16(w, hc[3], acc[f][3]);
    SCHED();
  }
}

__global__ __launch_bounds__(768, 3) void gemm2_kernel(const unsigned char* __restrict__ Hf, const unsigned char* __restrict__ wd,
                                                       unsigned char* __restrict__ out, unsigned char* __restrict__ ws_base) {
  __shared__ __attribute__((aligned(1024))) unsigned char smem[G2_LDS];
  const uint32_t lbase = (uint32_t)(uintptr_t)(LAS unsigned char*)smem;
  const int bid = blockIdx.x;
  const int xcd = bid & 7, idx = bid >> 3;
  const int kh2 = xcd & 1;
  const int rp = 4 * (xcd >> 1) + (idx & 3), cp = idx >> 2;
  const int lane = threadIdx.x & 63;
  const int wid = __builtin_amdgcn_readfirstlane(threadIdx.x >> 6);

  if (wid == 0) {
    const char* pc = (const char*)__builtin_amdgcn_s_getpc();
    l2pf(pc + (size_t)lane * 76, lbase + G2_RING);
  }
  if (wid >= 8) {
    const int l = wid - 8;
    const char* hs = (const char*)Hf + (size_t)(rp * 112 + kh2 * 56) * 8192 + l * 4096;
    const char* ws = (const char*)wd + (size_t)(kh2 * 1792 + 16 * l) * 2048 + cp * 256;
    const uint32_t hv = lane * 16;
    const int pc = lane & 15, r4 = lane >> 4;
    const uint32_t wv0 = r4 * 2048 + ((((pc >> 1) ^ r4) & 7) * 2 + (pc & 1)) * 16;
    const uint32_t wv1 = r4 * 2048 + ((((pc >> 1) ^ (4 + r4)) & 7) * 2 + (pc & 1)) * 16;
    const uint32_t hb = lbase + l * 4096, wb = lbase + 16384 + l * 4096;
    g2_issue(hs, hv, ws, wv0, wv1, 0, hb, wb);
    g2_issue(hs, hv, ws, wv0, wv1, 1, hb + G2_ST, wb + G2_ST);
    WAIT_VM(8);
    BAR();
    g2_issue(hs, hv, ws, wv0, wv1, 2, hb + 2 * G2_ST, wb + 2 * G2_ST);
    WAIT_VM(8);
    uint32_t so = 0;
    for (int t = 0; t < G2_NT - 3; ++t) {
      BAR();
      g2_issue(hs, hv, ws, wv0, wv1, t + 3, hb + so, wb + so);
      so += G2_ST; if (so == G2_RING) so = 0;
      WAIT_VM(8);
    }
    BAR();
    WAIT_VM(0);
    BAR();
    BAR();
    return;
  }

  const int wm = wid & 1, wn = (wid >> 1) & 1, kh = wid >> 2;
  uint32_t wa0[4];
  {
    const int g = lane >> 4, q = (lane >> 2) & 3, p = lane & 3;
    const int r = 4 * g + q;
#pragma unroll
    for (int f = 0; f < 4; ++f)
      wa0[f] = lbase + 16384 + (32 * kh + r) * 256 + (((4 * wn + f) ^ r) & 7) * 32 + p * 8;
  }
  const int own = 2 * kh + kh2;
  uint32_t ha0[4];
#pragma unroll
  for (int a = 0; a < 4; ++a) ha0[a] = lbase + kh * 8192 + wm * 4096 + (a ^ own) * 1024 + lane * 16;
  const uint32_t xw = lbase + G2_RING + wid * 8192 + lane * 16, xrd = lbase + G2_RING + (wid ^ 4) * 8192 + lane * 16;

  f32x4 acc[4][4];
#pragma unroll
  for (int f = 0; f < 4; ++f)
#pragma unroll
    for (int a = 0; a < 4; ++a) acc[f][a] = (f32x4){0.f, 0.f, 0.f, 0.f};
  bf16x8 hf0[4], hf1[4];
  bf16x4 wl0[4], wh0[4], wl1[4], wh1[4];

  BAR();
#pragma unroll
  for (int a = 0; a < 4; ++a) hf0[a] = lds_b128(ha0[a]);
#pragma unroll
  for (int f = 0; f < 4; ++f) { wl0[f] = lds_tr(wa0[f]); wh0[f] = lds_tr(wa0[f] + 4096); }
  uint32_t so = 0;
#define G2_STEP(HC, WLC, WHC, HN, WLN, WHN)                                                    \
  {                                                                                            \
    WAIT_LGKM0(); BAR();                                                                       \
    so += G2_ST; if (so == G2_RING) so = 0;                                                    \
    const uint32_t wa[4] = {wa0[0] + so, wa0[1] + so, wa0[2] + so, wa0[3] + so};               \
    const uint32_t ha[4] = {ha0[0] + so, ha0[1] + so, ha0[2] + so, ha0[3] + so};               \
    g2_step<true>(ha, wa, acc, HC, WLC, WHC, HN, WLN, WHN);                                    \
  }
  for (int t = 0; t < G2_NT - 2; t += 2) {
    G2_STEP(hf0, wl0, wh0, hf1, wl1, wh1);
    G2_STEP(hf1, wl1, wh1, hf0, wl0, wh0);
  }
  G2_STEP(hf0, wl0, wh0, hf1, wl1, wh1);
  g2_step<false>(ha0, wa0, acc, hf1, wl1, wh1, hf0, wl0, wh0);
#pragma unroll
  for (int f = 0; f < 4; ++f) *(LAS f32x4*)(xw + f * 1024) = acc[f][3];
#pragma unroll
  for (int f = 0; f < 4; ++f) *(LAS f32x4*)(xw + 4096 + f * 1024) = acc[f][2];
  WAIT_LGKM0();
  BAR();
  {
    unsigned char* xs = ws_base + WS_XCH + (size_t)((bid ^ 1) * 8 + wid) * 4096 + lane * 16;
#pragma unroll
    for (int f = 0; f < 4; ++f) {
      const f32x4 v = acc[f][1] + *(LAS f32x4*)(xrd + f * 1024);
      st16(xs + f * 1024, __builtin_bit_cast(u32x4, v));
    }
  }
#pragma unroll
  for (int f = 0; f < 4; ++f) acc[f][0] += *(LAS f32x4*)(xrd + 4096 + f * 1024);
  {
    WAIT_VM(0);
    unsigned* myflag = (unsigned*)(ws_base + WS_FLAGS + (size_t)(bid * 8 + wid) * 64);
    const unsigned* pflag = (const unsigned*)(ws_base + WS_FLAGS + (size_t)((bid ^ 1) * 8 + wid) * 64);
    if (lane == 0) __hip_atomic_store(myflag, 1u, __ATOMIC_RELAXED, __HIP_MEMORY_SCOPE_AGENT);
    for (int spin = 0; spin < (1 << 22); ++spin) {
      const unsigned v = __builtin_amdgcn_readfirstlane(__hip_atomic_load(pflag, __ATOMIC_RELAXED, __HIP_MEMORY_SCOPE_AGENT));
      if (v != 0) break;
      __builtin_amdgcn_s_sleep(1);
    }
    asm volatile("" ::: "memory");
    const unsigned char* xr = ws_base + WS_XCH + (size_t)(bid * 8 + wid) * 4096 + lane * 16;
    f32x4 o0, o1, o2, o3;
    asm volatile("global_load_dwordx4 %0, %4, off sc1\n\t"
                 "global_load_dwordx4 %1, %4, off offset:1024 sc1\n\t"
                 "global_load_dwordx4 %2, %4, off offset:2048 sc1\n\t"
                 "global_load_dwordx4 %3, %4, off offset:3072 sc1\n\t"
                 "s_waitcnt vmcnt(0)"
                 : "=&v"(o0), "=&v"(o1), "=&v"(o2), "=&v"(o3) : "v"(xr) : "memory");
    acc[0][0] += o0; acc[1][0] += o1; acc[2][0] += o2; acc[3][0] += o3;
  }
  {
    const int g = lane >> 4, r16 = lane & 15;
    unsigned char* op = out + (size_t)(rp * 128 + wm * 64 + own * 16 + r16) * 2048 + cp * 256 + wn * 128 + g * 8;
#pragma unroll
    for (int f = 0; f < 4; ++f) {
      const f32x4 v = acc[f][0];
      u32x2 pk; pk.x = pack2v((f32x2){v[0], v[1]}); pk.y = pack2v((f32x2){v[2], v[3]});
      *(u32x2*)(op + f * 32) = pk;
    }
  }
}

extern "C" void kernel_launch(void* const* d_in, const int* in_sizes, int n_in,
                              void* d_out, int out_size, void* d_ws, size_t ws_size,
                              hipStream_t stream) {
  const unsigned char* x = (const unsigned char*)d_in[0];
  const unsigned char* wg = (const unsigned char*)d_in[1];
  const unsigned char* wu = (const unsigned char*)d_in[2];
  const unsigned char* wd = (const unsigned char*)d_in[3];
  unsigned char* Hf = (unsigned char*)d_ws;
  gemm1_kernel<<<256, 512, 0, stream>>>(x, wg, wu, wd, Hf);
  gemm2_kernel<<<256, 768, 0, stream>>>(Hf, wd, (unsigned char*)d_out, (unsigned char*)d_ws);
}
```

```cpp
#include <hip/hip_runtime.h>
#include <hip/hip_bf16.h>
#include <stdint.h>

typedef __bf16 bf16x8 __attribute__((ext_vector_type(8)));
typedef __bf16 bf16x4 __attribute__((ext_vector_type(4)));
typedef __bf16 bf16x2 __attribute__((ext_vector_type(2)));
typedef float f32x4 __attribute__((ext_vector_type(4)));
typedef float f32x2 __attribute__((ext_vector_type(2)));
typedef unsigned u32x2 __attribute__((ext_vector_type(2)));
typedef unsigned u32x4 __attribute__((ext_vector_type(4)));
#define LAS __attribute__((address_space(3)))

#define S_ 2048
#define D_ 1024
#define F_ 3584

__device__ __forceinline__ void glds16(const void* sbase, uint32_t voff, uint32_t lds_addr) {
  asm volatile("s_mov_b32 m0, %2\n\ts_nop 0\n\tglobal_load_lds_dwordx4 %0, %1"
               :: "v"(voff), "s"(sbase), "s"(lds_addr) : "memory");
}
template <int OFF>
__device__ __forceinline__ void glds16_off(const void* sbase, uint32_t voff, uint32_t lds_addr) {
  asm volatile("s_mov_b32 m0, %2\n\ts_nop 0\n\tglobal_load_lds_dwordx4 %0, %1 offset:%3"
               :: "v"(voff), "s"(sbase), "s"(lds_addr), "n"(OFF) : "memory");
}
__device__ __forceinline__ void l2pf(const char* addr, uint32_t lds_scratch) {
  asm volatile("s_mov_b32 m0, %1\n\ts_nop 0\n\tglobal_load_lds_dword %0, off"
               :: "v"(addr), "s"(lds_scratch) : "memory");
}
#define WAIT_VM(n) asm volatile("s_waitcnt vmcnt(" #n ")" ::: "memory")
#define WAIT_LGKM0() asm volatile("s_waitcnt lgkmcnt(0)" ::: "memory")
#define BAR() do { asm volatile("" ::: "memory"); __builtin_amdgcn_s_barrier(); asm volatile("" ::: "memory"); } while (0)
#define SCHED() __builtin_amdgcn_sched_barrier(0)

__device__ __forceinline__ bf16x8 lds_b128(uint32_t addr) { return *(LAS bf16x8*)(addr); }
__device__ __forceinline__ bf16x4 lds_tr(uint32_t a) { return __builtin_amdgcn_ds_read_tr16_b64_v4bf16((LAS bf16x4*)(a)); }
__device__ __forceinline__ bf16x8 cat8(bf16x4 lo, bf16x4 hi) { return __builtin_shufflevector(lo, hi, 0, 1, 2, 3, 4, 5, 6, 7); }
__device__ __forceinline__ f32x4 mfma16(bf16x8 a, bf16x8 b, f32x4 c) {
  return __builtin_amdgcn_mfma_f32_16x16x32_bf16(a, b, c, 0, 0, 0);
}
__device__ __forceinline__ f32x2 unpack2(unsigned u) {
  f32x2 r; r.x = __builtin_bit_cast(float, u << 16); r.y = __builtin_bit_cast(float, u & 0xffff0000u); return r;
}
__device__ __forceinline__ unsigned pack2v(f32x2 v) {
  return __builtin_bit_cast(unsigned, __builtin_convertvector(v, bf16x2));
}

#define DBG_STAMP 0

constexpr int G1_XB = 32768;
constexpr int G1_WB = 28672;
constexpr int G1_WOFF = 3 * G1_XB;
constexpr int G1_SCR = 3 * G1_XB + 2 * G1_WB;
constexpr int G1_LDS = G1_SCR + 256;
#define G1_PD 4
#define PFMODE 0
#define CODE_PF_WAVES 3
#define CODE_PF_STRIDE 104
constexpr int G1_NT = D_ / 64;

struct G1Ctx {
  const char* xs;
  const char* ws[4];
  uint32_t xv[2];
  uint32_t wv[4];
  uint32_t xl;
  uint32_t wl[4];
  uint32_t xa[2];
  uint32_t wa[2];
  const char* pf;
  const char* pf2;
  uint32_t pfs2;
  uint32_t scr;
  bool pfw;
};

__device__ __forceinline__ void g1_issue_x(const G1Ctx& c, int idx, int t, uint32_t xoff) {
  glds16(c.xs + (idx >> 1) * (16 * 2048) + t * 128, c.xv[idx & 1], c.xl + xoff + idx * 1024);
}
__device__ __forceinline__ void g1_issue_w(const G1Ctx& c, int j, int t, uint32_t woff) {
  glds16(c.ws[j] + (size_t)t * (64 * F_ * 2), c.wv[j], c.wl[j] + woff);
}

template <int NF>
__device__ __forceinline__ void g1_tile(const G1Ctx& c, int t, uint32_t xo_cur, uint32_t xo_nxt, uint32_t xo_free, uint32_t wo_cur,
                                        f32x4 (&acc)[NF][4], bf16x8 (&xf)[2][4], bf16x4 (&wlo)[4], bf16x4 (&whi)[4],
                                        unsigned long long& st_wait) {
  constexpr int NWL = (NF == 8) ? 3 : 4;
  constexpr int NS = 2 * NF;
  const int t1 = (t + 1 < G1_NT) ? t + 1 : G1_NT - 1;
  const int t2 = (t + 2 < G1_NT) ? t + 2 : G1_NT - 1;
  const int tp = (t + G1_PD < G1_NT) ? t + G1_PD : G1_NT - 1;
  const uint32_t wo_nxt = G1_WB - wo_cur;
  const uint32_t xa1 = c.xa[1] + xo_cur;
  const uint32_t xa0n = c.xa[0] + xo_nxt;
  const uint32_t wac0 = c.wa[0] + wo_cur, wac1 = c.wa[1] + wo_cur;
  const uint32_t wan0 = c.wa[0] + wo_nxt, wan1 = c.wa[1] + wo_nxt;
#pragma unroll
  for (int j = 0; j < NS; ++j) {
    const int s = j / NF, f = j % NF;
    if (j == NS - 2) {
      WAIT_LGKM0();
      if (c.pfw) WAIT_VM(6); else WAIT_VM(4);
      BAR();
    }
    uint32_t a0, a1;
    if (j + 2 < NS) {
      const int s2 = (j + 2) / NF, f2 = (j + 2) % NF;
      const uint32_t o = s2 * (32 * 224) + (f2 & 1) * 14336 + (f2 >> 1) * 32;
      a0 = wac0 + o; a1 = wac1 + o;
    } else {
      const int f2 = j + 2 - NS;
      const uint32_t o = (f2 & 1) * 14336 + (f2 >> 1) * 32;
      a0 = wan0 + o; a1 = wan1 + o;
    }
    const bf16x8 w = cat8(wlo[j & 3], whi[j & 3]);
    acc[f][0] = mfma16(w, xf[s][0], acc[f][0]);
    SCHED();
    wlo[(j + 2) & 3] = lds_tr(a0);
    SCHED();
    acc[f][1] = mfma16(w, xf[s][1], acc[f][1]);
    SCHED();
    whi[(j + 2) & 3] = lds_tr(a1);
    SCHED();
    acc[f][2] = mfma16(w, xf[s][2], acc[f][2]);
    SCHED();
    if (j < 4) xf[1][j] = lds_b128(xa1 + j * 2048);
    if (j >= NS - 2) {
      const int m0 = 2 * (j - (NS - 2));
      xf[0][m0] = lds_b128(xa0n + m0 * 2048);
      xf[0][m0 + 1] = lds_b128(xa0n + (m0 + 1) * 2048);
    }
    SCHED();
    acc[f][3] = mfma16(w, xf[s][3], acc[f][3]);
    SCHED();
    if (j >= NS - 2) g1_issue_w(c, j - (NS - 2), t2, wo_cur);
    if (j + 2 < NWL) g1_issue_w(c, j + 2, t1, wo_nxt);
    if (j == 2 && c.pfw) { l2pf(c.pf + (size_t)(64 * F_ * 2) * tp, c.scr); l2pf(c.pf2 + (size_t)c.pfs2 * tp, c.scr); }
    if (j >= 3 && j < 7) g1_issue_x(c, j - 3, t2, xo_free);
    SCHED();
  }
}

#define EPI_ROUND 0
__device__ __forceinline__ unsigned geglu2(f32x2 g, f32x2 u) {
  const float c1 = -2.0f * 0.7978845608028654f * 1.4426950408889634f;
  const float c2 = c1 * 0.044715f;
  f32x2 p = (g * g) * c2 + c1;
  f32x2 a = g * p;
  f32x2 e; e.x = __builtin_amdgcn_exp2f(a.x); e.y = __builtin_amdgcn_exp2f(a.y);
  f32x2 d = e + 1.0f;
  f32x2 r; r.x = __builtin_amdgcn_rcpf(d.x); r.y = __builtin_amdgcn_rcpf(d.y);
  f32x2 ge = g * r;
  return pack2v(ge * u);
}
__device__ __forceinline__ void st16(unsigned char* p, u32x4 v) {
  asm volatile("global_store_dwordx4 %0, %1, off sc1\n\ts_nop 1" :: "v"(p), "v"(v) : "memory");
}
__device__ __forceinline__ void st8(unsigned char* p, u32x2 v) {
  asm volatile("global_store_dwordx2 %0, %1, off sc1" :: "v"(p), "v"(v) : "memory");
}
__device__ __forceinline__ uint32_t hkb(int kb) { return kb * 8192; }

template <int NP>
__device__ __forceinline__ void g1_store(unsigned char* hb, int nfg0, int i, const u32x2 (&hp)[NP][4]) {
  if (NP == 4) {
    if (i & 1) {
      unsigned char* p = hb + hkb((nfg0 + i) >> 1);
#pragma unroll
      for (int m = 0; m < 4; ++m)
        st16(p + m * 1024, (u32x4){hp[i - 1][m].x, hp[i - 1][m].y, hp[i][m].x, hp[i][m].y});
    }
  } else {
    if (i == 2) {
      if (nfg0 & 1) {
        unsigned char* p = hb + hkb(nfg0 >> 1) + 8;
        unsigned char* q = hb + hkb((nfg0 + 1) >> 1);
#pragma unroll
        for (int m = 0; m < 4; ++m) {
          st8(p + m * 1024, hp[0][m]);
          st16(q + m * 1024, (u32x4){hp[1][m].x, hp[1][m].y, hp[2][m].x, hp[2][m].y});
        }
      } else {
        unsigned char* p = hb + hkb((nfg0 + 2) >> 1);
        unsigned char* q = hb + hkb(nfg0 >> 1);
#pragma unroll
        for (int m = 0; m < 4; ++m) {
          st8(p + m * 1024, hp[2][m]);
          st16(q + m * 1024, (u32x4){hp[0][m].x, hp[0][m].y, hp[1][m].x, hp[1][m].y});
        }
      }
    }
  }
}

template <int NF>
__device__ __forceinline__ void g1_epi(f32x4 (&acc)[NF][4], unsigned char* hb, int nfg0) {
  constexpr int NP = NF / 2;
  u32x2 hp[NP][4];
#pragma unroll
  for (int i = 0; i < NP; ++i) {
#pragma unroll
    for (int e = 0; e < 4; ++e) {
      hp[i][e].x = geglu2((f32x2){acc[2 * i][e][0], acc[2 * i][e][1]}, (f32x2){acc[2 * i + 1][e][0], acc[2 * i + 1][e][1]});
      hp[i][e].y = geglu2((f32x2){acc[2 * i][e][2], acc[2 * i][e][3]}, (f32x2){acc[2 * i + 1][e][2], acc[2 * i + 1][e][3]});
    }
    g1_store<NP>(hb, nfg0, i, hp);
  }
}

template <int NF>
__device__ __forceinline__ void g1_last(const G1Ctx& c, uint32_t xo_cur, uint32_t wo_cur, f32x4 (&acc)[NF][4], bf16x8 (&xf)[2][4],
                                        bf16x4 (&wlo)[4], bf16x4 (&whi)[4], unsigned char* hb, int nfg0) {
  constexpr int NP = NF / 2;
  constexpr int NS = 2 * NF;
  const uint32_t xa1 = c.xa[1] + xo_cur;
  const uint32_t wac0 = c.wa[0] + wo_cur, wac1 = c.wa[1] + wo_cur;
  u32x2 hp[NP][4];
#pragma unroll
  for (int j = 0; j < NS; ++j) {
    const int i = j >> 2, s = (j >> 1) & 1, f = 2 * i + (j & 1);
    const int j2 = j + 2, i2 = j2 >> 2, s2 = (j2 >> 1) & 1;
    const uint32_t o = s2 * (32 * 224) + (j2 & 1) * 14336 + i2 * 32;
    const bf16x8 w = cat8(wlo[j & 3], whi[j & 3]);
    const int e = j & 3;
    acc[f][0] = mfma16(w, xf[s][0], acc[f][0]);
    SCHED();
    if (j2 < NS) wlo[j2 & 3] = lds_tr(wac0 + o);
    if (j < 2) xf[1][2 * j] = lds_b128(xa1 + (2 * j) * 2048);
    SCHED();
    acc[f][1] = mfma16(w, xf[s][1], acc[f][1]);
    SCHED();
    if (j2 < NS) whi[j2 & 3] = lds_tr(wac1 + o);
    if (j < 2) xf[1][2 * j + 1] = lds_b128(xa1 + (2 * j + 1) * 2048);
    if (i > 0) hp[i - 1][e].x = geglu2((f32x2){acc[2 * i - 2][e][0], acc[2 * i - 2][e][1]}, (f32x2){acc[2 * i - 1][e][0], acc[2 * i - 1][e][1]});
    SCHED();
    acc[f][2] = mfma16(w, xf[s][2], acc[f][2]);
    SCHED();
    if (i > 0) hp[i - 1][e].y = geglu2((f32x2){acc[2 * i - 2][e][2], acc[2 * i - 2][e][3]}, (f32x2){acc[2 * i - 1][e][2], acc[2 * i - 1][e][3]});
    SCHED();
    acc[f][3] = mfma16(w, xf[s][3], acc[f][3]);
    SCHED();
    if (i > 0 && e == 3) g1_store<NP>(hb, nfg0, i - 1, hp);
    SCHED();
  }
#pragma unroll
  for (int e = 0; e < 4; ++e) {
    hp[NP - 1][e].x = geglu2((f32x2){acc[NF - 2][e][0], acc[NF - 2][e][1]}, (f32x2){acc[NF - 1][e][0], acc[NF - 1][e][1]});
    hp[NP - 1][e].y = geglu2((f32x2){acc[NF - 2][e][2], acc[NF - 2][e][3]}, (f32x2){acc[NF - 1][e][2], acc[NF - 1][e][3]});
  }
  g1_store<NP>(hb, nfg0, NP - 1, hp);
}

constexpr size_t WS_XCH = (size_t)32 << 20;
constexpr size_t WS_FLAGS = (size_t)16 << 20;

template <int NF>
__device__ __forceinline__ void g1_run(const G1Ctx& c, unsigned char* hb, unsigned char* xch, int nfg0, int lane) {
  constexpr int NWL = (NF == 8) ? 3 : 4;
  unsigned long long st_wait = 0;
  f32x4 acc[NF][4];
#pragma unroll
  for (int f = 0; f < NF; ++f)
#pragma unroll
    for (int m = 0; m < 4; ++m) acc[f][m] = (f32x4){0.f, 0.f, 0.f, 0.f};
  bf16x8 xf[2][4];
  bf16x4 wlo[4], whi[4];
#pragma unroll
  for (int i = 0; i < 4; ++i) g1_issue_x(c, i, 0, 0);
#pragma unroll
  for (int i = 0; i < NWL; ++i) g1_issue_w(c, i, 0, 0);
#pragma unroll
  for (int i = 0; i < 2; ++i) g1_issue_w(c, i, 1, G1_WB);
  WAIT_VM(2);
  BAR();
#pragma unroll
  for (int m = 0; m < 4; ++m) xf[0][m] = lds_b128(c.xa[0] + m * 2048);
  wlo[0] = lds_tr(c.wa[0]); whi[0] = lds_tr(c.wa[1]);
  wlo[1] = lds_tr(c.wa[0] + 14336); whi[1] = lds_tr(c.wa[1] + 14336);
  SCHED();
#pragma unroll
  for (int i = 0; i < 4; ++i) g1_issue_x(c, i, 1, G1_XB);
  if (c.pfw) {
#pragma unroll
    for (int i = 2; i < G1_PD; ++i) { l2pf(c.pf + (size_t)(64 * F_ * 2) * i, c.scr); l2pf(c.pf2 + (size_t)c.pfs2 * i, c.scr); }
  }
  SCHED();
  uint32_t xo0 = 0, xo1 = G1_XB, xo2 = 2 * G1_XB, wo = 0;
#define LAST_SEP 0
#pragma nounroll
  for (int t = 0; t < G1_NT - LAST_SEP; ++t) {
    g1_tile<NF>(c, t, xo0, xo1, xo2, wo, acc, xf, wlo, whi, st_wait);
    const uint32_t tmp = xo0; xo0 = xo1; xo1 = xo2; xo2 = tmp;
    wo = G1_WB - wo;
  }
  g1_epi<NF>(acc, hb, nfg0);
}

constexpr int G2_NT = 28;
constexpr int G2_ST = 32768;
constexpr int G2_RING = 3 * G2_ST;
constexpr size_t WS_SLOTS = (size_t)17 << 20;

template <int OFF>
__device__ __forceinline__ void glds16_sc1(const void* sbase, uint32_t voff, uint32_t lds_addr) {
  asm volatile("s_mov_b32 m0, %2\n\ts_nop 0\n\tglobal_load_lds_dwordx4 %0, %1 offset:%3 sc1"
               :: "v"(voff), "s"(sbase), "s"(lds_addr), "n"(OFF) : "memory");
}
__device__ __forceinline__ void g2_issue_h(const char* hs, uint32_t hv, int st, uint32_t hdst) {
  const char* hp = hs + (size_t)st * 16384;
  glds16_sc1<0>(hp, hv, hdst);
  glds16_sc1<1024>(hp, hv, hdst);
  glds16_sc1<2048>(hp, hv, hdst);
  glds16_sc1<3072>(hp, hv, hdst);
}
__device__ __forceinline__ void g2_issue_w(const char* ws, uint32_t wv0, uint32_t wv1, int st, uint32_t wdst) {
  const char* wp = ws + (size_t)st * (64 * 2048);
  glds16(wp, wv0, wdst);
  glds16(wp + 4 * 2048, wv1, wdst + 1024);
  glds16(wp + 8 * 2048, wv0, wdst + 2048);
  glds16(wp + 12 * 2048, wv1, wdst + 3072);
}

template <bool RD>
__device__ __forceinline__ void g2_step(const uint32_t (&ha)[2], const uint32_t (&wa)[4], f32x4 (&acc)[4][4],
                                        const bf16x8 (&hc)[8], const bf16x4 (&wlc)[8], const bf16x4 (&whc)[8],
                                        bf16x8 (&hn)[8], bf16x4 (&wln)[8], bf16x4 (&whn)[8]) {
#pragma unroll
  for (int k = 0; k < 2; ++k)
#pragma unroll
    for (int f = 0; f < 4; ++f) {
      const int i = 4 * k + f;
      const bf16x8 w = cat8(wlc[i], whc[i]);
      acc[f][0] = mfma16(w, hc[4 * k + 0], acc[f][0]);
      SCHED();
      if (RD) wln[i] = lds_tr(wa[f] + k * 8192);
      SCHED();
      acc[f][1] = mfma16(w, hc[4 * k + 1], acc[f][1]);
      SCHED();
      if (RD) whn[i] = lds_tr(wa[f] + k * 8192 + 4096);
      SCHED();
      acc[f][2] = mfma16(w, hc[4 * k + 2], acc[f][2]);
      SCHED();
      if (RD) hn[i] = lds_b128(ha[f & 1] + k * 8192 + (f >> 1) * 2048);
      SCHED();
      acc[f][3] = mfma16(w, hc[4 * k + 3], acc[f][3]);
      SCHED();
    }
}

__global__ __launch_bounds__(512, 2) void ffn_kernel(const unsigned char* __restrict__ x, const unsigned char* __restrict__ wg,
                                                     const unsigned char* __restrict__ wu, const unsigned char* __restrict__ wd,
                                                     unsigned char* __restrict__ out, unsigned char* __restrict__ Hf) {
  __shared__ __attribute__((aligned(1024))) unsigned char smem[G1_LDS];
  const uint32_t lbase = (uint32_t)(uintptr_t)(LAS unsigned char*)smem;
  const int bid = blockIdx.x;
  const int xcd = bid & 7, idx = bid >> 3;
  const int pm = 4 * (xcd & 1) + (idx & 3), pn = 8 * (xcd >> 1) + (idx >> 2);
  const int brow = pm * 256;
  const int lane = threadIdx.x & 63;
  const int wid = __builtin_amdgcn_readfirstlane(threadIdx.x >> 6);
  const int wr = wid & 3, wc = wid >> 2;
  if (wid < CODE_PF_WAVES) {
    const char* pc = (const char*)__builtin_amdgcn_s_getpc();
    l2pf(pc + (size_t)(wid * 64 + lane) * CODE_PF_STRIDE, lbase + G1_SCR);
  }
  unsigned* myslots = (unsigned*)(Hf + WS_SLOTS) + (size_t)(pm * 32 + pn) * 32;
  if (wid == 0) {
    if (lane < 32) __hip_atomic_store(myslots + lane, 0u, __ATOMIC_RELAXED, __HIP_MEMORY_SCOPE_AGENT);
    else if (lane < 36) __hip_atomic_store((unsigned*)(Hf + WS_FLAGS + (size_t)(bid * 4 + lane - 32) * 64), 0u, __ATOMIC_RELAXED, __HIP_MEMORY_SCOPE_AGENT);
  }

  {
    G1Ctx c;
    c.xs = (const char*)x + (size_t)(brow + 32 * wid) * 2048;
#pragma unroll
    for (int a = 0; a < 2; ++a) {
      const int r = 8 * a + (lane >> 3);
      const int ch = (lane & 7) ^ ((4 * a + (lane >> 4)) & 7);
      c.xv[a] = r * 2048 + ch * 16;
    }
    c.xl = lbase + wid * 4096;
    const int e0 = (wid < 4) ? 3 * wid : 12 + 4 * (wid - 4);
#pragma unroll
    for (int j = 0; j < 4; ++j) {
      int e = e0 + j; if (e > 27) e = 27;
      const int mat = e / 14, jj = e % 14;
      const int ci = 64 * jj + lane;
      const int rowp = ci / 14, cc = ci % 14;
      const int row = rowp ^ (((rowp >> 3) & 1) << 2);
      c.wv[j] = row * (F_ * 2) + cc * 16;
      c.ws[j] = (const char*)(mat ? wu : wg) + pn * 224;
      c.wl[j] = lbase + G1_WOFF + mat * 14336 + jj * 1024;
    }
    {
      c.pfw = (wid == 4);
      const int li = idx * 56 + (lane < 56 ? lane : 0);
      const int mat = li / 896, r = (li % 896) / 14, cc = li % 14;
      c.pf = (const char*)(mat ? wu : wg) + (size_t)r * (F_ * 2) + (xcd >> 1) * 1792 + cc * 128;
      if (lane >= 32 && lane < 48) {
        c.pf2 = (const char*)wd + (size_t)((bid * 16 + (lane - 32)) * 128) - (size_t)2 * (256 * 2048);
        c.pfs2 = 256 * 2048;
      } else {
        c.pf2 = (const char*)x + (size_t)(brow + 32 * (pn & 7) + (lane & 31)) * 2048;
        c.pfs2 = 128;
      }
      c.scr = lbase + G1_SCR;
    }
    const int g = lane >> 4, r16 = lane & 15;
#pragma unroll
    for (int s = 0; s < 2; ++s)
      c.xa[s] = lbase + (wr * 64 + r16) * 128 + (((4 * s + g) ^ (r16 >> 1)) & 7) * 16;
    const int q = r16 >> 2, p = lane & 3;
    const int fbase = (wc == 0) ? ((pn & 1) ? 3 : 0) : ((pn & 1) ? 0 : 4);
#pragma unroll
    for (int i = 0; i < 2; ++i)
      c.wa[i] = lbase + G1_WOFF + (8 * g + 4 * (i ^ (g & 1)) + q) * 224 + fbase * 32 + p * 8;
    const int nfg0 = 7 * pn + fbase;
    unsigned char* hb = Hf + (size_t)(2 * pm + (wr >> 1)) * (112 * 8192) + (wr & 1) * 4096 + lane * 16;
    if (wc == 0) g1_run<8>(c, hb, nullptr, nfg0, lane);
    else g1_run<6>(c, hb, nullptr, nfg0, lane);
  }
  WAIT_LGKM0();
  WAIT_VM(0);
  BAR();

  const int kh2 = (pn >> 3) & 1, cp = ((pn >> 4) << 2) | ((pn >> 1) & 3), rp = 2 * pm + (pn & 1);
  if (wc == 1) {
    const int l = wr;
    const char* hs = (const char*)Hf + (size_t)(rp * 112 + kh2 * 56) * 8192 + l * 4096;
    const char* ws = (const char*)wd + (size_t)(kh2 * 1792 + 16 * l) * 2048 + cp * 256;
    const uint32_t hv = lane * 16;
    const int pc = lane & 15, r4 = lane >> 4;
    const uint32_t wv0 = r4 * 2048 + ((((pc >> 1) ^ r4) & 7) * 2 + (pc & 1)) * 16;
    const uint32_t wv1 = r4 * 2048 + ((((pc >> 1) ^ (4 + r4)) & 7) * 2 + (pc & 1)) * 16;
    const uint32_t hb = lbase + l * 4096, wb = lbase + 16384 + l * 4096;
#pragma unroll
    for (int i = 0; i < 3; ++i) g2_issue_w(ws, wv0, wv1, i, wb + i * G2_ST);
    BAR();
    g2_issue_h(hs, hv, 0, hb);
    g2_issue_h(hs, hv, 1, hb + G2_ST);
    WAIT_VM(4);
    BAR();
    g2_issue_h(hs, hv, 2, hb + 2 * G2_ST);
    WAIT_VM(4);
    uint32_t so = 0;
    for (int t = 0; t < G2_NT - 3; ++t) {
      BAR();
      g2_issue_h(hs, hv, t + 3, hb + so);
      g2_issue_w(ws, wv0, wv1, t + 3, wb + so);
      so += G2_ST; if (so == G2_RING) so = 0;
      WAIT_VM(8);
    }
    BAR();
    WAIT_VM(0);
    BAR();
    return;
  }

  if (wid == 0) {
    unsigned* dst = (unsigned*)(Hf + WS_SLOTS) + (size_t)(pm * 32 + (lane & 31)) * 32 + pn;
    if (lane < 32) __hip_atomic_store(dst, 1u, __ATOMIC_RELAXED, __HIP_MEMORY_SCOPE_AGENT);
    for (int spin = 0; spin < (1 << 20); ++spin) {
      const unsigned v = __hip_atomic_load(myslots + (lane & 31), __ATOMIC_RELAXED, __HIP_MEMORY_SCOPE_AGENT);
      const bool done = __all(v == 1u);
      asm volatile("" ::: "memory");
      if (lane < 32) __hip_atomic_store(dst, 1u, __ATOMIC_RELAXED, __HIP_MEMORY_SCOPE_AGENT);
      if (done) break;
      __builtin_amdgcn_s_sleep(1);
    }
    asm volatile("" ::: "memory");
  }
  BAR();

  const int wm = wid & 1, wn = wid >> 1;
  uint32_t wa0[4];
  {
    const int g = lane >> 4, q = (lane >> 2) & 3, p = lane & 3;
    const int r = 4 * g + q;
#pragma unroll
    for (int f = 0; f < 4; ++f)
      wa0[f] = lbase + 16384 + r * 256 + (((4 * wn + f) ^ r) & 7) * 32 + p * 8;
  }
  uint32_t ha0[2];
#pragma unroll
  for (int a = 0; a < 2; ++a) ha0[a] = lbase + wm * 4096 + (a ^ kh2) * 1024 + lane * 16;

  f32x4 acc[4][4];
#pragma unroll
  for (int f = 0; f < 4; ++f)
#pragma unroll
    for (int a = 0; a < 4; ++a) acc[f][a] = (f32x4){0.f, 0.f, 0.f, 0.f};
  bf16x8 hf0[8], hf1[8];
  bf16x4 wl0[8], wh0[8], wl1[8], wh1[8];

  BAR();
#pragma unroll
  for (int i = 0; i < 8; ++i) hf0[i] = lds_b128(ha0[i & 1] + (i >> 2) * 8192 + ((i >> 1) & 1) * 2048);
#pragma unroll
  for (int i = 0; i < 8; ++i) { wl0[i] = lds_tr(wa0[i & 3] + (i >> 2) * 8192); wh0[i] = lds_tr(wa0[i & 3] + (i >> 2) * 8192 + 4096); }
  uint32_t so = 0;
#define G2_STEP(HC, WLC, WHC, HN, WLN, WHN)                                                    \
  {                                                                                            \
    WAIT_LGKM0(); BAR();                                                                       \
    so += G2_ST; if (so == G2_RING) so = 0;                                                    \
    const uint32_t wa[4] = {wa0[0] + so, wa0[1] + so, wa0[2] + so, wa0[3] + so};               \
    const uint32_t ha[2] = {ha0[0] + so, ha0[1] + so};                                         \
    g2_step<true>(ha, wa, acc, HC, WLC, WHC, HN, WLN, WHN);                                    \
  }
  for (int t = 0; t < G2_NT - 2; t += 2) {
    G2_STEP(hf0, wl0, wh0, hf1, wl1, wh1);
    G2_STEP(hf1, wl1, wh1, hf0, wl0, wh0);
  }
  G2_STEP(hf0, wl0, wh0, hf1, wl1, wh1);
  g2_step<false>(ha0, wa0, acc, hf1, wl1, wh1, hf0, wl0, wh0);
  const int pbid = bid ^ 2;
  {
    unsigned char* xs = Hf + WS_XCH + (size_t)(pbid * 4 + wid) * 8192 + lane * 16;
#pragma unroll
    for (int f = 0; f < 4; ++f) {
      st16(xs + f * 1024, __builtin_bit_cast(u32x4, acc[f][1]));
      st16(xs + 4096 + f * 1024, __builtin_bit_cast(u32x4, acc[f][3]));
    }
    WAIT_VM(0);
    unsigned* oflag = (unsigned*)(Hf + WS_FLAGS + (size_t)(pbid * 4 + wid) * 64);
    const unsigned* iflag = (const unsigned*)(Hf + WS_FLAGS + (size_t)(bid * 4 + wid) * 64);
    if (lane == 0) __hip_atomic_store(oflag, 1u, __ATOMIC_RELAXED, __HIP_MEMORY_SCOPE_AGENT);
    for (int spin = 0; spin < (1 << 22); ++spin) {
      const unsigned v = __builtin_amdgcn_readfirstlane(__hip_atomic_load(iflag, __ATOMIC_RELAXED, __HIP_MEMORY_SCOPE_AGENT));
      if (v == 1u) break;
      __builtin_amdgcn_s_sleep(1);
    }
    asm volatile("" ::: "memory");
    const unsigned char* xr = Hf + WS_XCH + (size_t)(bid * 4 + wid) * 8192 + lane * 16;
    f32x4 o[8];
    asm volatile("global_load_dwordx4 %0, %8, off sc1\n\t"
                 "global_load_dwordx4 %1, %8, off offset:1024 sc1\n\t"
                 "global_load_dwordx4 %2, %8, off offset:2048 sc1\n\t"
                 "global_load_dwordx4 %3, %8, off offset:3072 sc1\n\t"
                 "global_load_dwordx4 %4, %9, off sc1\n\t"
                 "global_load_dwordx4 %5, %9, off offset:1024 sc1\n\t"
                 "global_load_dwordx4 %6, %9, off offset:2048 sc1\n\t"
                 "global_load_dwordx4 %7, %9, off offset:3072 sc1\n\t"
                 "s_waitcnt vmcnt(0)"
                 : "=&v"(o[0]), "=&v"(o[1]), "=&v"(o[2]), "=&v"(o[3]), "=&v"(o[4]), "=&v"(o[5]), "=&v"(o[6]), "=&v"(o[7])
                 : "v"(xr), "v"(xr + 4096) : "memory");
#pragma unroll
    for (int f = 0; f < 4; ++f) { acc[f][0] += o[f]; acc[f][2] += o[4 + f]; }
  }
  {
    const int g = lane >> 4, r16 = lane & 15;
    unsigned char* op = out + (size_t)(rp * 128 + wm * 64 + kh2 * 16 + r16) * 2048 + cp * 256 + wn * 128 + g * 8;
#pragma unroll
    for (int a = 0; a < 2; ++a)
#pragma unroll
      for (int f = 0; f < 4; ++f) {
        const f32x4 v = acc[f][2 * a];
        u32x2 pk; pk.x = pack2v((f32x2){v[0], v[1]}); pk.y = pack2v((f32x2){v[2], v[3]});
        *(u32x2*)(op + (size_t)a * (32 * 2048) + f * 32) = pk;
      }
  }
}

extern "C" void kernel_launch(void* const* d_in, const int* in_sizes, int n_in,
                              void* d_out, int out_size, void* d_ws, size_t ws_size,
                              hipStream_t stream) {
  const unsigned char* x = (const unsigned char*)d_in[0];
  const unsigned char* wg = (const unsigned char*)d_in[1];
  const unsigned char* wu = (const unsigned char*)d_in[2];
  const unsigned char* wd = (const unsigned char*)d_in[3];
  ffn_kernel<<<256, 512, 0, stream>>>(x, wg, wu, wd, (unsigned char*)d_out, (unsigned char*)d_ws);
}
```

```cpp
#include <hip/hip_runtime.h>
#include <hip/hip_bf16.h>
#include <stdint.h>

typedef __bf16 bf16x8 __attribute__((ext_vector_type(8)));
typedef __bf16 bf16x4 __attribute__((ext_vector_type(4)));
typedef __bf16 bf16x2 __attribute__((ext_vector_type(2)));
typedef float f32x4 __attribute__((ext_vector_type(4)));
typedef float f32x2 __attribute__((ext_vector_type(2)));
typedef unsigned u32x2 __attribute__((ext_vector_type(2)));
typedef unsigned u32x4 __attribute__((ext_vector_type(4)));
#define LAS __attribute__((address_space(3)))

#define S_ 2048
#define D_ 1024
#define F_ 3584

__device__ __forceinline__ void glds16(const void* sbase, uint32_t voff, uint32_t lds_addr) {
  asm volatile("s_mov_b32 m0, %2\n\ts_nop 0\n\tglobal_load_lds_dwordx4 %0, %1"
               :: "v"(voff), "s"(sbase), "s"(lds_addr) : "memory");
}
template <int OFF>
__device__ __forceinline__ void glds16_off(const void* sbase, uint32_t voff, uint32_t lds_addr) {
  asm volatile("s_mov_b32 m0, %2\n\ts_nop 0\n\tglobal_load_lds_dwordx4 %0, %1 offset:%3"
               :: "v"(voff), "s"(sbase), "s"(lds_addr), "n"(OFF) : "memory");
}
__device__ __forceinline__ void l2pf(const char* addr, uint32_t lds_scratch) {
  asm volatile("s_mov_b32 m0, %1\n\ts_nop 0\n\tglobal_load_lds_dword %0, off"
               :: "v"(addr), "s"(lds_scratch) : "memory");
}
#define WAIT_VM(n) asm volatile("s_waitcnt vmcnt(" #n ")" ::: "memory")
#define WAIT_LGKM0() asm volatile("s_waitcnt lgkmcnt(0)" ::: "memory")
#define BAR() do { asm volatile("" ::: "memory"); __builtin_amdgcn_s_barrier(); asm volatile("" ::: "memory"); } while (0)
#define SCHED() __builtin_amdgcn_sched_barrier(0)

__device__ __forceinline__ bf16x8 lds_b128(uint32_t addr) { return *(LAS bf16x8*)(addr); }
__device__ __forceinline__ bf16x4 lds_tr(uint32_t a) { return __builtin_amdgcn_ds_read_tr16_b64_v4bf16((LAS bf16x4*)(a)); }
__device__ __forceinline__ bf16x8 cat8(bf16x4 lo, bf16x4 hi) { return __builtin_shufflevector(lo, hi, 0, 1, 2, 3, 4, 5, 6, 7); }
__device__ __forceinline__ f32x4 mfma16(bf16x8 a, bf16x8 b, f32x4 c) {
  return __builtin_amdgcn_mfma_f32_16x16x32_bf16(a, b, c, 0, 0, 0);
}
__device__ __forceinline__ f32x2 unpack2(unsigned u) {
  f32x2 r; r.x = __builtin_bit_cast(float, u << 16); r.y = __builtin_bit_cast(float, u & 0xffff0000u); return r;
}
__device__ __forceinline__ unsigned pack2v(f32x2 v) {
  return __builtin_bit_cast(unsigned, __builtin_convertvector(v, bf16x2));
}

#define DBG_STAMP 0

constexpr int G1_XB = 32768;
constexpr int G1_WB = 28672;
constexpr int G1_WOFF = 3 * G1_XB;
constexpr int G1_SCR = 3 * G1_XB + 2 * G1_WB;
constexpr int G1_LDS = G1_SCR + 256;
#define G1_PD 4
#define PFMODE 0
#define EPI_WARM 1
#define EARLY_PF 2
#define CODE_PF_WAVES 3
#define CODE_PF_STRIDE 100
constexpr int G1_NT = D_ / 64;

struct G1Ctx {
  const char* xs;
  const char* ws[4];
  uint32_t xv[2];
  uint32_t wv[4];
  uint32_t xl;
  uint32_t wl[4];
  uint32_t xa[2];
  uint32_t wa[2];
  const char* pf;
  const char* pf2;
  uint32_t pfs2;
  uint32_t scr;
  bool pfw;
};

__device__ __forceinline__ void g1_issue_x(const G1Ctx& c, int idx, int t, uint32_t xoff) {
  glds16(c.xs + (idx >> 1) * (16 * 2048) + t * 128, c.xv[idx & 1], c.xl + xoff + idx * 1024);
}
__device__ __forceinline__ void g1_issue_w(const G1Ctx& c, int j, int t, uint32_t woff) {
  glds16(c.ws[j] + (size_t)t * (64 * F_ * 2), c.wv[j], c.wl[j] + woff);
}

template <int NF>
__device__ __forceinline__ void g1_tile(const G1Ctx& c, int t, uint32_t xo_cur, uint32_t xo_nxt, uint32_t xo_free, uint32_t wo_cur,
                                        f32x4 (&acc)[NF][4], bf16x8 (&xf)[2][4], bf16x4 (&wlo)[4], bf16x4 (&whi)[4],
                                        unsigned long long& st_wait) {
  constexpr int NWL = (NF == 8) ? 3 : 4;
  constexpr int NS = 2 * NF;
  const int t1 = (t + 1 < G1_NT) ? t + 1 : G1_NT - 1;
  const int t2 = (t + 2 < G1_NT) ? t + 2 : G1_NT - 1;
  const int tp = (t + G1_PD < G1_NT) ? t + G1_PD : G1_NT - 1;
  const uint32_t wo_nxt = G1_WB - wo_cur;
  const uint32_t xa1 = c.xa[1] + xo_cur;
  const uint32_t xa0n = c.xa[0] + xo_nxt;
  const uint32_t wac0 = c.wa[0] + wo_cur, wac1 = c.wa[1] + wo_cur;
  const uint32_t wan0 = c.wa[0] + wo_nxt, wan1 = c.wa[1] + wo_nxt;
#pragma unroll
  for (int j = 0; j < NS; ++j) {
    const int s = j / NF, f = j % NF;
    if (j == NS - 2) {
      WAIT_LGKM0();
      if (c.pfw) WAIT_VM(6); else WAIT_VM(4);
      BAR();
    }
    uint32_t a0, a1;
    if (j + 2 < NS) {
      const int s2 = (j + 2) / NF, f2 = (j + 2) % NF;
      const uint32_t o = s2 * (32 * 224) + (f2 & 1) * 14336 + (f2 >> 1) * 32;
      a0 = wac0 + o; a1 = wac1 + o;
    } else {
      const int f2 = j + 2 - NS;
      const uint32_t o = (f2 & 1) * 14336 + (f2 >> 1) * 32;
      a0 = wan0 + o; a1 = wan1 + o;
    }
    const bf16x8 w = cat8(wlo[j & 3], whi[j & 3]);
    acc[f][0] = mfma16(w, xf[s][0], acc[f][0]);
    SCHED();
    wlo[(j + 2) & 3] = lds_tr(a0);
    SCHED();
    acc[f][1] = mfma16(w, xf[s][1], acc[f][1]);
    SCHED();
    whi[(j + 2) & 3] = lds_tr(a1);
    SCHED();
    acc[f][2] = mfma16(w, xf[s][2], acc[f][2]);
    SCHED();
    if (j < 4) xf[1][j] = lds_b128(xa1 + j * 2048);
    if (j >= NS - 2) {
      const int m0 = 2 * (j - (NS - 2));
      xf[0][m0] = lds_b128(xa0n + m0 * 2048);
      xf[0][m0 + 1] = lds_b128(xa0n + (m0 + 1) * 2048);
    }
    SCHED();
    acc[f][3] = mfma16(w, xf[s][3], acc[f][3]);
    SCHED();
    if (j >= NS - 2) g1_issue_w(c, j - (NS - 2), t2, wo_cur);
    if (j + 2 < NWL) g1_issue_w(c, j + 2, t1, wo_nxt);
    if (j == 2 && c.pfw) { l2pf(c.pf + (size_t)(64 * F_ * 2) * tp, c.scr); l2pf(c.pf2 + (size_t)c.pfs2 * tp, c.scr); }
    if (j >= 3 && j < 7) g1_issue_x(c, j - 3, t2, xo_free);
    SCHED();
  }
}

#define EPI_ROUND 0
__device__ __forceinline__ unsigned geglu2(f32x2 g, f32x2 u) {
  const float c1 = -2.0f * 0.7978845608028654f * 1.4426950408889634f;
  const float c2 = c1 * 0.044715f;
  f32x2 p = (g * g) * c2 + c1;
  f32x2 a = g * p;
  f32x2 e; e.x = __builtin_amdgcn_exp2f(a.x); e.y = __builtin_amdgcn_exp2f(a.y);
  f32x2 d = e + 1.0f;
  f32x2 r; r.x = __builtin_amdgcn_rcpf(d.x); r.y = __builtin_amdgcn_rcpf(d.y);
  f32x2 ge = g * r;
  return pack2v(ge * u);
}
__device__ __forceinline__ void st16(unsigned char* p, u32x4 v) {
  asm volatile("global_store_dwordx4 %0, %1, off sc1\n\ts_nop 1" :: "v"(p), "v"(v) : "memory");
}
__device__ __forceinline__ void st8(unsigned char* p, u32x2 v) {
  asm volatile("global_store_dwordx2 %0, %1, off sc1" :: "v"(p), "v"(v) : "memory");
}
__device__ __forceinline__ uint32_t hkb(int kb) { return kb * 8192; }

template <int NP>
__device__ __forceinline__ void g1_store(unsigned char* hb, int nfg0, int i, const u32x2 (&hp)[NP][4]) {
  if (NP == 4) {
    if (i & 1) {
      unsigned char* p = hb + hkb((nfg0 + i) >> 1);
#pragma unroll
      for (int m = 0; m < 4; ++m)
        st16(p + m * 1024, (u32x4){hp[i - 1][m].x, hp[i - 1][m].y, hp[i][m].x, hp[i][m].y});
    }
  } else {
    if (i == 2) {
      if (nfg0 & 1) {
        unsigned char* p = hb + hkb(nfg0 >> 1) + 8;
        unsigned char* q = hb + hkb((nfg0 + 1) >> 1);
#pragma unroll
        for (int m = 0; m < 4; ++m) {
          st8(p + m * 1024, hp[0][m]);
          st16(q + m * 1024, (u32x4){hp[1][m].x, hp[1][m].y, hp[2][m].x, hp[2][m].y});
        }
      } else {
        unsigned char* p = hb + hkb((nfg0 + 2) >> 1);
        unsigned char* q = hb + hkb(nfg0 >> 1);
#pragma unroll
        for (int m = 0; m < 4; ++m) {
          st8(p + m * 1024, hp[2][m]);
          st16(q + m * 1024, (u32x4){hp[0][m].x, hp[0][m].y, hp[1][m].x, hp[1][m].y});
        }
      }
    }
  }
}

template <int NF>
__device__ __forceinline__ void g1_epi(f32x4 (&acc)[NF][4], unsigned char* hb, int nfg0, bool do_store) {
  constexpr int NP = NF / 2;
  u32x2 hp[NP][4];
#pragma unroll
  for (int i = 0; i < NP; ++i) {
#pragma unroll
    for (int e = 0; e < 4; ++e) {
      hp[i][e].x = geglu2((f32x2){acc[2 * i][e][0], acc[2 * i][e][1]}, (f32x2){acc[2 * i + 1][e][0], acc[2 * i + 1][e][1]});
      hp[i][e].y = geglu2((f32x2){acc[2 * i][e][2], acc[2 * i][e][3]}, (f32x2){acc[2 * i + 1][e][2], acc[2 * i + 1][e][3]});
    }
    if (do_store) g1_store<NP>(hb, nfg0, i, hp);
    else {
#pragma unroll
      for (int e = 0; e < 4; ++e) asm volatile("" :: "v"(hp[i][e]));
    }
  }
}

template <int NF>
__device__ __forceinline__ void g1_last(const G1Ctx& c, uint32_t xo_cur, uint32_t wo_cur, f32x4 (&acc)[NF][4], bf16x8 (&xf)[2][4],
                                        bf16x4 (&wlo)[4], bf16x4 (&whi)[4], unsigned char* hb, int nfg0) {
  constexpr int NP = NF / 2;
  constexpr int NS = 2 * NF;
  const uint32_t xa1 = c.xa[1] + xo_cur;
  const uint32_t wac0 = c.wa[0] + wo_cur, wac1 = c.wa[1] + wo_cur;
  u32x2 hp[NP][4];
#pragma unroll
  for (int j = 0; j < NS; ++j) {
    const int i = j >> 2, s = (j >> 1) & 1, f = 2 * i + (j & 1);
    const int j2 = j + 2, i2 = j2 >> 2, s2 = (j2 >> 1) & 1;
    const uint32_t o = s2 * (32 * 224) + (j2 & 1) * 14336 + i2 * 32;
    const bf16x8 w = cat8(wlo[j & 3], whi[j & 3]);
    const int e = j & 3;
    if (j2 < NS) { wlo[j2 & 3] = lds_tr(wac0 + o); whi[j2 & 3] = lds_tr(wac1 + o); }
    if (j < 2) { xf[1][2 * j] = lds_b128(xa1 + (2 * j) * 2048); xf[1][2 * j + 1] = lds_b128(xa1 + (2 * j + 1) * 2048); }
#pragma unroll
    for (int m = 0; m < 4; ++m) acc[f][m] = mfma16(w, xf[s][m], acc[f][m]);
    if (i > 0) {
      hp[i - 1][e].x = geglu2((f32x2){acc[2 * i - 2][e][0], acc[2 * i - 2][e][1]}, (f32x2){acc[2 * i - 1][e][0], acc[2 * i - 1][e][1]});
      hp[i - 1][e].y = geglu2((f32x2){acc[2 * i - 2][e][2], acc[2 * i - 2][e][3]}, (f32x2){acc[2 * i - 1][e][2], acc[2 * i - 1][e][3]});
#pragma unroll
      for (int m = 0; m < 4; ++m) {
        __builtin_amdgcn_sched_group_barrier(0x008, 1, 0);
        __builtin_amdgcn_sched_group_barrier(0x002, 6, 0);
      }
    }
    if (i > 0 && e == 3) g1_store<NP>(hb, nfg0, i - 1, hp);
    SCHED();
  }
#pragma unroll
  for (int e = 0; e < 4; ++e) {
    hp[NP - 1][e].x = geglu2((f32x2){acc[NF - 2][e][0], acc[NF - 2][e][1]}, (f32x2){acc[NF - 1][e][0], acc[NF - 1][e][1]});
    hp[NP - 1][e].y = geglu2((f32x2){acc[NF - 2][e][2], acc[NF - 2][e][3]}, (f32x2){acc[NF - 1][e][2], acc[NF - 1][e][3]});
  }
  g1_store<NP>(hb, nfg0, NP - 1, hp);
}

constexpr size_t WS_XCH = (size_t)32 << 20;
constexpr size_t WS_FLAGS = (size_t)16 << 20;

template <int NF>
__device__ __forceinline__ void g1_run(const G1Ctx& c, unsigned char* hb, unsigned char* xch, int nfg0, int lane) {
  constexpr int NWL = (NF == 8) ? 3 : 4;
  unsigned long long st_wait = 0;
  f32x4 acc[NF][4];
#pragma unroll
  for (int f = 0; f < NF; ++f)
#pragma unroll
    for (int m = 0; m < 4; ++m) acc[f][m] = (f32x4){0.f, 0.f, 0.f, 0.f};
  bf16x8 xf[2][4];
  bf16x4 wlo[4], whi[4];
#pragma unroll
  for (int i = 0; i < 4; ++i) g1_issue_x(c, i, 0, 0);
#pragma unroll
  for (int i = 0; i < NWL; ++i) g1_issue_w(c, i, 0, 0);
#pragma unroll
  for (int i = 0; i < 2; ++i) g1_issue_w(c, i, 1, G1_WB);
  WAIT_VM(2);
  BAR();
#pragma unroll
  for (int m = 0; m < 4; ++m) xf[0][m] = lds_b128(c.xa[0] + m * 2048);
  wlo[0] = lds_tr(c.wa[0]); whi[0] = lds_tr(c.wa[1]);
  wlo[1] = lds_tr(c.wa[0] + 14336); whi[1] = lds_tr(c.wa[1] + 14336);
  SCHED();
#pragma unroll
  for (int i = 0; i < 4; ++i) g1_issue_x(c, i, 1, G1_XB);
  if (c.pfw) {
#pragma unroll
    for (int i = 2; i < G1_PD; ++i) { l2pf(c.pf + (size_t)(64 * F_ * 2) * i, c.scr); l2pf(c.pf2 + (size_t)c.pfs2 * i, c.scr); }
  }
  SCHED();
  uint32_t xo0 = 0, xo1 = G1_XB, xo2 = 2 * G1_XB, wo = 0;
#define LAST_SEP 0
#pragma nounroll
  for (int t = 0; t < G1_NT - LAST_SEP; ++t) {
    g1_tile<NF>(c, t, xo0, xo1, xo2, wo, acc, xf, wlo, whi, st_wait);
    const uint32_t tmp = xo0; xo0 = xo1; xo1 = xo2; xo2 = tmp;
    wo = G1_WB - wo;
  }
  g1_epi<NF>(acc, hb, nfg0, true);
}

constexpr int G2_NT = 28;
constexpr int G2_ST = 32768;
constexpr int G2_RING = 3 * G2_ST;
constexpr size_t WS_SLOTS = (size_t)17 << 20;

template <int OFF>
__device__ __forceinline__ void glds16_sc1(const void* sbase, uint32_t voff, uint32_t lds_addr) {
  asm volatile("s_mov_b32 m0, %2\n\ts_nop 0\n\tglobal_load_lds_dwordx4 %0, %1 offset:%3 sc1"
               :: "v"(voff), "s"(sbase), "s"(lds_addr), "n"(OFF) : "memory");
}
__device__ __forceinline__ void g2_issue_h(const char* hs, uint32_t hv, int st, uint32_t hdst) {
  const char* hp = hs + (size_t)st * 16384;
  glds16_sc1<0>(hp, hv, hdst);
  glds16_sc1<1024>(hp, hv, hdst);
  glds16_sc1<2048>(hp, hv, hdst);
  glds16_sc1<3072>(hp, hv, hdst);
}
__device__ __forceinline__ void g2_issue_w(const char* ws, uint32_t wv0, uint32_t wv1, int st, uint32_t wdst) {
  const char* wp = ws + (size_t)st * (64 * 2048);
  glds16(wp, wv0, wdst);
  glds16(wp + 4 * 2048, wv1, wdst + 1024);
  glds16(wp + 8 * 2048, wv0, wdst + 2048);
  glds16(wp + 12 * 2048, wv1, wdst + 3072);
}
__device__ __forceinline__ unsigned g2_need(int st) {
  const int lo = (64 * st) / 112, hi = (64 * st + 63) / 112;
  return (1u << lo) | (1u << hi);
}

template <bool RD>
__device__ __forceinline__ void g2_kb(const uint32_t (&ha)[2], const uint32_t (&wa)[4], uint32_t off, f32x4 (&acc)[4][4],
                                      const bf16x8 (&hc)[4], const bf16x4 (&wlc)[4], const bf16x4 (&whc)[4],
                                      bf16x8 (&hn)[4], bf16x4 (&wln)[4], bf16x4 (&whn)[4]) {
#pragma unroll
  for (int f = 0; f < 4; ++f) {
    const bf16x8 w = cat8(wlc[f], whc[f]);
    acc[f][0] = mfma16(w, hc[0], acc[f][0]);
    SCHED();
    if (RD) wln[f] = lds_tr(wa[f] + off);
    SCHED();
    acc[f][1] = mfma16(w, hc[1], acc[f][1]);
    SCHED();
    if (RD) whn[f] = lds_tr(wa[f] + off + 4096);
    SCHED();
    acc[f][2] = mfma16(w, hc[2], acc[f][2]);
    SCHED();
    if (RD) hn[f] = lds_b128(ha[f & 1] + off + (f >> 1) * 2048);
    SCHED();
    acc[f][3] = mfma16(w, hc[3], acc[f][3]);
    SCHED();
  }
}

__device__ __forceinline__ void g2_finish(unsigned char* ws_base, int bid, int pbid, int wid, int lane, const f32x4 (&snd)[4], f32x4 (&kep)[4],
                                          unsigned char* op) {
  unsigned char* xs = ws_base + WS_XCH + (size_t)(pbid * 8 + wid) * 4096 + lane * 16;
#pragma unroll
  for (int f = 0; f < 4; ++f) st16(xs + f * 1024, __builtin_bit_cast(u32x4, snd[f]));
  WAIT_VM(0);
  unsigned* oflag = (unsigned*)(ws_base + WS_FLAGS + (size_t)(pbid * 8 + wid) * 64);
  const unsigned* iflag = (const unsigned*)(ws_base + WS_FLAGS + (size_t)(bid * 8 + wid) * 64);
  if (lane == 0) __hip_atomic_store(oflag, 1u, __ATOMIC_RELAXED, __HIP_MEMORY_SCOPE_AGENT);
  for (int spin = 0; spin < (1 << 22); ++spin) {
    const unsigned v = __builtin_amdgcn_readfirstlane(__hip_atomic_load(iflag, __ATOMIC_RELAXED, __HIP_MEMORY_SCOPE_AGENT));
    if (v == 1u) break;
    __builtin_amdgcn_s_sleep(1);
  }
  asm volatile("" ::: "memory");
  const unsigned char* xr = ws_base + WS_XCH + (size_t)(bid * 8 + wid) * 4096 + lane * 16;
  f32x4 o0, o1, o2, o3;
  asm volatile("global_load_dwordx4 %0, %4, off sc1\n\t"
               "global_load_dwordx4 %1, %4, off offset:1024 sc1\n\t"
               "global_load_dwordx4 %2, %4, off offset:2048 sc1\n\t"
               "global_load_dwordx4 %3, %4, off offset:3072 sc1\n\t"
               "s_waitcnt vmcnt(0)"
               : "=&v"(o0), "=&v"(o1), "=&v"(o2), "=&v"(o3) : "v"(xr) : "memory");
  kep[0] += o0; kep[1] += o1; kep[2] += o2; kep[3] += o3;
#pragma unroll
  for (int f = 0; f < 4; ++f) {
    const f32x4 v = kep[f];
    u32x2 pk; pk.x = pack2v((f32x2){v[0], v[1]}); pk.y = pack2v((f32x2){v[2], v[3]});
    *(u32x2*)(op + f * 32) = pk;
  }
}

__global__ __launch_bounds__(512, 2) void ffn_kernel(const unsigned char* __restrict__ x, const unsigned char* __restrict__ wg,
                                                     const unsigned char* __restrict__ wu, const unsigned char* __restrict__ wd,
                                                     unsigned char* __restrict__ out, unsigned char* __restrict__ Hf) {
  __shared__ __attribute__((aligned(1024))) unsigned char smem[G1_LDS];
  const uint32_t lbase = (uint32_t)(uintptr_t)(LAS unsigned char*)smem;
  const int bid = blockIdx.x;
  const int xcd = bid & 7, idx = bid >> 3;
  const int pm = 4 * (xcd & 1) + (idx & 3), pn = 8 * (xcd >> 1) + (idx >> 2);
  const int brow = pm * 256;
  const int lane = threadIdx.x & 63;
  const int wid = __builtin_amdgcn_readfirstlane(threadIdx.x >> 6);
  const int wr = wid & 3, wc = wid >> 2;
  const char* pfa; const char* pfb; uint32_t pfbs;
  {
    const int li = idx * 56 + (lane < 56 ? lane : 0);
    const int mat = li / 896, r = (li % 896) / 14, cc = li % 14;
    pfa = (const char*)(mat ? wu : wg) + (size_t)r * (F_ * 2) + (xcd >> 1) * 1792 + cc * 128;
    const char* px = (const char*)x + (size_t)(brow + 32 * (pn & 7) + (lane & 31)) * 2048;
    const int t0 = wid >> 1;
    if (t0 < EARLY_PF) {
      if (wid & 1) l2pf(pfa + (size_t)(64 * F_ * 2) * t0, lbase + G1_SCR);
      else l2pf(px + 128 * t0, lbase + G1_SCR);
    }
    if (lane >= 32 && lane < 48) {
      pfb = (const char*)wd + (size_t)((bid * 16 + (lane - 32)) * 128) - (size_t)2 * (256 * 2048);
      pfbs = 256 * 2048;
    } else {
      pfb = px;
      pfbs = 128;
    }
  }
  if (wid < CODE_PF_WAVES) {
    const char* pc = (const char*)__builtin_amdgcn_s_getpc();
    l2pf(pc + (size_t)(wid * 64 + lane) * CODE_PF_STRIDE, lbase + G1_SCR);
  }
  unsigned* myslots = (unsigned*)(Hf + WS_SLOTS) + (size_t)(pm * 32 + pn) * 32;
  if (wid == 4) {
    if (lane < 32) __hip_atomic_store(myslots + lane, 0u, __ATOMIC_RELAXED, __HIP_MEMORY_SCOPE_AGENT);
    else if (lane < 40) __hip_atomic_store((unsigned*)(Hf + WS_FLAGS + (size_t)(bid * 8 + lane - 32) * 64), 0u, __ATOMIC_RELAXED, __HIP_MEMORY_SCOPE_AGENT);
  }

  {
    G1Ctx c;
    c.xs = (const char*)x + (size_t)(brow + 32 * wid) * 2048;
#pragma unroll
    for (int a = 0; a < 2; ++a) {
      const int r = 8 * a + (lane >> 3);
      const int ch = (lane & 7) ^ ((4 * a + (lane >> 4)) & 7);
      c.xv[a] = r * 2048 + ch * 16;
    }
    c.xl = lbase + wid * 4096;
    const int e0 = (wid < 4) ? 3 * wid : 12 + 4 * (wid - 4);
#pragma unroll
    for (int j = 0; j < 4; ++j) {
      int e = e0 + j; if (e > 27) e = 27;
      const int mat = e / 14, jj = e % 14;
      const int ci = 64 * jj + lane;
      const int rowp = ci / 14, cc = ci % 14;
      const int row = rowp ^ (((rowp >> 3) & 1) << 2);
      c.wv[j] = row * (F_ * 2) + cc * 16;
      c.ws[j] = (const char*)(mat ? wu : wg) + pn * 224;
      c.wl[j] = lbase + G1_WOFF + mat * 14336 + jj * 1024;
    }
    c.pfw = (wid == 4);
    c.pf = pfa; c.pf2 = pfb; c.pfs2 = pfbs;
    c.scr = lbase + G1_SCR;
    const int g = lane >> 4, r16 = lane & 15;
#pragma unroll
    for (int s = 0; s < 2; ++s)
      c.xa[s] = lbase + (wr * 64 + r16) * 128 + (((4 * s + g) ^ (r16 >> 1)) & 7) * 16;
    const int q = r16 >> 2, p = lane & 3;
    const int fbase = (wc == 0) ? ((pn & 1) ? 3 : 0) : ((pn & 1) ? 0 : 4);
#pragma unroll
    for (int i = 0; i < 2; ++i)
      c.wa[i] = lbase + G1_WOFF + (8 * g + 4 * (i ^ (g & 1)) + q) * 224 + fbase * 32 + p * 8;
    const int nfg0 = 7 * pn + fbase;
    unsigned char* hb = Hf + (size_t)(2 * pm + (wr >> 1)) * (112 * 8192) + (wr & 1) * 4096 + lane * 16;
    if (wc == 0) g1_run<8>(c, hb, nullptr, nfg0, lane);
    else g1_run<6>(c, hb, nullptr, nfg0, lane);
  }
  WAIT_LGKM0();
  WAIT_VM(0);
  BAR();

  const int kh2 = pn >> 4, cp = ((pn >> 3) & 1) * 4 + ((pn >> 1) & 3), rp = 2 * pm + (pn & 1);
  const int s0 = ((112 * (pn & 15) + 63) / 64) % G2_NT;
  const int pbid = bid ^ 4;
  const int wm = wr & 1, wn = wr >> 1;
  unsigned char* op = out + (size_t)(rp * 128 + wm * 64 + wc * 32 + kh2 * 16 + (lane & 15)) * 2048 + cp * 256 + wn * 128 + (lane >> 4) * 8;
  const uint32_t xl = lbase + G2_RING + wr * 8192 + lane * 16;
  if (wc == 1) {
    const int l = wr;
    const char* hs = (const char*)Hf + (size_t)(rp * 112 + kh2 * 56) * 8192 + l * 4096;
    const char* ws = (const char*)wd + (size_t)(kh2 * 1792 + 16 * l) * 2048 + cp * 256;
    const uint32_t hv = lane * 16;
    const int pc = lane & 15, r4 = lane >> 4;
    const uint32_t wv0 = r4 * 2048 + ((((pc >> 1) ^ r4) & 7) * 2 + (pc & 1)) * 16;
    const uint32_t wv1 = r4 * 2048 + ((((pc >> 1) ^ (4 + r4)) & 7) * 2 + (pc & 1)) * 16;
    const uint32_t hb = lbase + l * 4096, wb = lbase + 16384 + l * 4096;
    unsigned* dst = (unsigned*)(Hf + WS_SLOTS) + (size_t)(pm * 32 + (lane & 31)) * 32 + pn;
    unsigned ready = 1u << pn;
#define G2_WAIT_READY(NEED)                                                                                              \
    if (l == 0) {                                                                                                        \
      const unsigned need_ = (NEED);                                                                                     \
      for (int spin = 0; (ready & need_) != need_ && spin < (1 << 20); ++spin) {                                         \
        const unsigned v = __hip_atomic_load(myslots + (lane & 31), __ATOMIC_RELAXED, __HIP_MEMORY_SCOPE_AGENT);         \
        ready |= (unsigned)__ballot(v == 1u);                                                                            \
      }                                                                                                                  \
      asm volatile("" ::: "memory");                                                                                     \
    }
    if (l == 0 && lane < 32) __hip_atomic_store(dst, 1u, __ATOMIC_RELAXED, __HIP_MEMORY_SCOPE_AGENT);
    int sa = s0, sb = s0 + 1, sc = s0 + 2;
    if (sb >= G2_NT) sb -= G2_NT;
    if (sc >= G2_NT) sc -= G2_NT;
    g2_issue_h(hs, hv, sa, hb);
    g2_issue_w(ws, wv0, wv1, sa, wb);
    g2_issue_w(ws, wv0, wv1, sb, wb + G2_ST);
    g2_issue_w(ws, wv0, wv1, sc, wb + 2 * G2_ST);
    G2_WAIT_READY((g2_need(sb) | g2_need(sc)) << (16 * kh2));
    WAIT_VM(8);
    BAR();
    g2_issue_h(hs, hv, sb, hb + G2_ST);
    g2_issue_h(hs, hv, sc, hb + 2 * G2_ST);
    int st = sc;
    uint32_t so = 0;
    for (int t = 0; t < G2_NT - 3; ++t) {
      ++st; if (st >= G2_NT) st -= G2_NT;
      G2_WAIT_READY(g2_need(st) << (16 * kh2));
      if (t == 0) WAIT_VM(4); else WAIT_VM(8);
      BAR();
      g2_issue_h(hs, hv, st, hb + so);
      g2_issue_w(ws, wv0, wv1, st, wb + so);
      so += G2_ST; if (so == G2_RING) so = 0;
    }
    WAIT_VM(8);
    BAR();
    WAIT_VM(0);
    BAR();
    G2_WAIT_READY(0xffffffffu);
    if (l == 0 && lane < 32) __hip_atomic_store(dst, 1u, __ATOMIC_RELAXED, __HIP_MEMORY_SCOPE_AGENT);
    BAR();
    f32x4 kep[4], snd[4];
#pragma unroll
    for (int f = 0; f < 4; ++f) { snd[f] = *(LAS f32x4*)(xl + f * 1024); kep[f] = *(LAS f32x4*)(xl + 4096 + f * 1024); }
    g2_finish(Hf, bid, pbid, wid, lane, snd, kep, op);
    return;
  }

  uint32_t wa0[4];
  {
    const int g = lane >> 4, q = (lane >> 2) & 3, p = lane & 3;
    const int r = 4 * g + q;
#pragma unroll
    for (int f = 0; f < 4; ++f)
      wa0[f] = lbase + 16384 + r * 256 + (((4 * wn + f) ^ r) & 7) * 32 + p * 8;
  }
  uint32_t ha0[2];
#pragma unroll
  for (int a = 0; a < 2; ++a) ha0[a] = lbase + wm * 4096 + (a ^ kh2) * 1024 + lane * 16;

  f32x4 acc[4][4];
#pragma unroll
  for (int f = 0; f < 4; ++f)
#pragma unroll
    for (int a = 0; a < 4; ++a) acc[f][a] = (f32x4){0.f, 0.f, 0.f, 0.f};
  bf16x8 hf0[4], hf1[4];
  bf16x4 wl0[4], wh0[4], wl1[4], wh1[4];

  BAR();
#pragma unroll
  for (int i = 0; i < 4; ++i) hf0[i] = lds_b128(ha0[i & 1] + (i >> 1) * 2048);
#pragma unroll
  for (int i = 0; i < 4; ++i) { wl0[i] = lds_tr(wa0[i]); wh0[i] = lds_tr(wa0[i] + 4096); }
  uint32_t so = 0;
  for (int t = 0; t < G2_NT - 1; ++t) {
    {
      const uint32_t wa[4] = {wa0[0] + so, wa0[1] + so, wa0[2] + so, wa0[3] + so};
      const uint32_t ha[2] = {ha0[0] + so, ha0[1] + so};
      g2_kb<true>(ha, wa, 8192, acc, hf0, wl0, wh0, hf1, wl1, wh1);
    }
    WAIT_LGKM0();
    BAR();
    so += G2_ST; if (so == G2_RING) so = 0;
    {
      const uint32_t wa[4] = {wa0[0] + so, wa0[1] + so, wa0[2] + so, wa0[3] + so};
      const uint32_t ha[2] = {ha0[0] + so, ha0[1] + so};
      g2_kb<true>(ha, wa, 0, acc, hf1, wl1, wh1, hf0, wl0, wh0);
    }
  }
  {
    const uint32_t wa[4] = {wa0[0] + so, wa0[1] + so, wa0[2] + so, wa0[3] + so};
    const uint32_t ha[2] = {ha0[0] + so, ha0[1] + so};
    g2_kb<true>(ha, wa, 8192, acc, hf0, wl0, wh0, hf1, wl1, wh1);
    g2_kb<false>(ha, wa, 0, acc, hf1, wl1, wh1, hf0, wl0, wh0);
  }
#pragma unroll
  for (int f = 0; f < 4; ++f) { *(LAS f32x4*)(xl + f * 1024) = acc[f][3]; *(LAS f32x4*)(xl + 4096 + f * 1024) = acc[f][2]; }
  WAIT_LGKM0();
  BAR();
  {
    f32x4 kep[4], snd[4];
#pragma unroll
    for (int f = 0; f < 4; ++f) { snd[f] = acc[f][1]; kep[f] = acc[f][0]; }
    g2_finish(Hf, bid, pbid, wid, lane, snd, kep, op);
  }
}

extern "C" void kernel_launch(void* const* d_in, const int* in_sizes, int n_in,
                              void* d_out, int out_size, void* d_ws, size_t ws_size,
                              hipStream_t stream) {
  const unsigned char* x = (const unsigned char*)d_in[0];
  const unsigned char* wg = (const unsigned char*)d_in[1];
  const unsigned char* wu = (const unsigned char*)d_in[2];
  const unsigned char* wd = (const unsigned char*)d_in[3];
  ffn_kernel<<<256, 512, 0, stream>>>(x, wg, wu, wd, (unsigned char*)d_out, (unsigned char*)d_ws);
}
```

```cpp
#include <hip/hip_runtime.h>
#include <hip/hip_bf16.h>
#include <stdint.h>

typedef __bf16 bf16x8 __attribute__((ext_vector_type(8)));
typedef __bf16 bf16x4 __attribute__((ext_vector_type(4)));
typedef __bf16 bf16x2 __attribute__((ext_vector_type(2)));
typedef float f32x4 __attribute__((ext_vector_type(4)));
typedef float f32x2 __attribute__((ext_vector_type(2)));
typedef unsigned u32x2 __attribute__((ext_vector_type(2)));
typedef unsigned u32x4 __attribute__((ext_vector_type(4)));
#define LAS __attribute__((address_space(3)))

#define S_ 2048
#define D_ 1024
#define F_ 3584

__device__ __forceinline__ void glds16(const void* sbase, uint32_t voff, uint32_t lds_addr) {
  asm volatile("s_mov_b32 m0, %2\n\ts_nop 0\n\tglobal_load_lds_dwordx4 %0, %1"
               :: "v"(voff), "s"(sbase), "s"(lds_addr) : "memory");
}
template <int OFF>
__device__ __forceinline__ void glds16_off(const void* sbase, uint32_t voff, uint32_t lds_addr) {
  asm volatile("s_mov_b32 m0, %2\n\ts_nop 0\n\tglobal_load_lds_dwordx4 %0, %1 offset:%3"
               :: "v"(voff), "s"(sbase), "s"(lds_addr), "n"(OFF) : "memory");
}
__device__ __forceinline__ void l2pf(const char* addr, uint32_t lds_scratch) {
  asm volatile("s_mov_b32 m0, %1\n\ts_nop 0\n\tglobal_load_lds_dword %0, off"
               :: "v"(addr), "s"(lds_scratch) : "memory");
}
#define WAIT_VM(n) asm volatile("s_waitcnt vmcnt(" #n ")" ::: "memory")
#define WAIT_LGKM0() asm volatile("s_waitcnt lgkmcnt(0)" ::: "memory")
#define BAR() do { asm volatile("" ::: "memory"); __builtin_amdgcn_s_barrier(); asm volatile("" ::: "memory"); } while (0)
#define SCHED() __builtin_amdgcn_sched_barrier(0)

__device__ __forceinline__ bf16x8 lds_b128(uint32_t addr) { return *(LAS bf16x8*)(addr); }
__device__ __forceinline__ bf16x4 lds_tr(uint32_t a) { return __builtin_amdgcn_ds_read_tr16_b64_v4bf16((LAS bf16x4*)(a)); }
__device__ __forceinline__ bf16x8 cat8(bf16x4 lo, bf16x4 hi) { return __builtin_shufflevector(lo, hi, 0, 1, 2, 3, 4, 5, 6, 7); }
__device__ __forceinline__ f32x4 mfma16(bf16x8 a, bf16x8 b, f32x4 c) {
  return __builtin_amdgcn_mfma_f32_16x16x32_bf16(a, b, c, 0, 0, 0);
}
__device__ __forceinline__ f32x2 unpack2(unsigned u) {
  f32x2 r; r.x = __builtin_bit_cast(float, u << 16); r.y = __builtin_bit_cast(float, u & 0xffff0000u); return r;
}
__device__ __forceinline__ unsigned pack2v(f32x2 v) {
  return __builtin_bit_cast(unsigned, __builtin_convertvector(v, bf16x2));
}

#define DBG_STAMP 0

constexpr int G1_XB = 32768;
constexpr int G1_WB = 28672;
constexpr int G1_WOFF = 3 * G1_XB;
constexpr int G1_SCR = 3 * G1_XB + 2 * G1_WB;
constexpr int G1_LDS = G1_SCR + 256;
#define G1_PD 4
#define PFMODE 0
#define EPI_WARM 1
#define EARLY_PF 2
#define CODE_PF_WAVES 3
#define CODE_PF_STRIDE 100
constexpr int G1_NT = D_ / 64;

struct G1Ctx {
  const char* xs;
  const char* ws[4];
  uint32_t xv[2];
  uint32_t wv[4];
  uint32_t xl;
  uint32_t wl[4];
  uint32_t xa[2];
  uint32_t wa[2];
  const char* pf;
  const char* pf2;
  uint32_t pfs2;
  uint32_t scr;
  bool pfw;
};

__device__ __forceinline__ void g1_issue_x(const G1Ctx& c, int idx, int t, uint32_t xoff) {
  glds16(c.xs + (idx >> 1) * (16 * 2048) + t * 128, c.xv[idx & 1], c.xl + xoff + idx * 1024);
}
__device__ __forceinline__ void g1_issue_w(const G1Ctx& c, int j, int t, uint32_t woff) {
  glds16(c.ws[j] + (size_t)t * (64 * F_ * 2), c.wv[j], c.wl[j] + woff);
}

template <int NF>
__device__ __forceinline__ void g1_tile(const G1Ctx& c, int t, uint32_t xo_cur, uint32_t xo_nxt, uint32_t xo_free, uint32_t wo_cur,
                                        f32x4 (&acc)[NF][4], bf16x8 (&xf)[2][4], bf16x4 (&wlo)[4], bf16x4 (&whi)[4],
                                        unsigned long long& st_wait) {
  constexpr int NWL = (NF == 8) ? 3 : 4;
  constexpr int NS = 2 * NF;
  const int t1 = (t + 1 < G1_NT) ? t + 1 : G1_NT - 1;
  const int t2 = (t + 2 < G1_NT) ? t + 2 : G1_NT - 1;
  const int tp = (t + G1_PD < G1_NT) ? t + G1_PD : G1_NT - 1;
  const uint32_t wo_nxt = G1_WB - wo_cur;
  const uint32_t xa1 = c.xa[1] + xo_cur;
  const uint32_t xa0n = c.xa[0] + xo_nxt;
  const uint32_t wac0 = c.wa[0] + wo_cur, wac1 = c.wa[1] + wo_cur;
  const uint32_t wan0 = c.wa[0] + wo_nxt, wan1 = c.wa[1] + wo_nxt;
#pragma unroll
  for (int j = 0; j < NS; ++j) {
    const int s = j / NF, f = j % NF;
    if (j == NS - 2) {
      WAIT_LGKM0();
      if (c.pfw) WAIT_VM(6); else WAIT_VM(4);
      BAR();
    }
    uint32_t a0, a1;
    if (j + 2 < NS) {
      const int s2 = (j + 2) / NF, f2 = (j + 2) % NF;
      const uint32_t o = s2 * (32 * 224) + (f2 & 1) * 14336 + (f2 >> 1) * 32;
      a0 = wac0 + o; a1 = wac1 + o;
    } else {
      const int f2 = j + 2 - NS;
      const uint32_t o = (f2 & 1) * 14336 + (f2 >> 1) * 32;
      a0 = wan0 + o; a1 = wan1 + o;
    }
    const bf16x8 w = cat8(wlo[j & 3], whi[j & 3]);
    acc[f][0] = mfma16(w, xf[s][0], acc[f][0]);
    SCHED();
    wlo[(j + 2) & 3] = lds_tr(a0);
    SCHED();
    acc[f][1] = mfma16(w, xf[s][1], acc[f][1]);
    SCHED();
    whi[(j + 2) & 3] = lds_tr(a1);
    SCHED();
    acc[f][2] = mfma16(w, xf[s][2], acc[f][2]);
    SCHED();
    if (j < 4) xf[1][j] = lds_b128(xa1 + j * 2048);
    if (j >= NS - 2) {
      const int m0 = 2 * (j - (NS - 2));
      xf[0][m0] = lds_b128(xa0n + m0 * 2048);
      xf[0][m0 + 1] = lds_b128(xa0n + (m0 + 1) * 2048);
    }
    SCHED();
    acc[f][3] = mfma16(w, xf[s][3], acc[f][3]);
    SCHED();
    if (j >= NS - 2) g1_issue_w(c, j - (NS - 2), t2, wo_cur);
    if (j + 2 < NWL) g1_issue_w(c, j + 2, t1, wo_nxt);
    if (j == 2 && c.pfw) { l2pf(c.pf + (size_t)(64 * F_ * 2) * tp, c.scr); l2pf(c.pf2 + (size_t)c.pfs2 * tp, c.scr); }
    if (j >= 3 && j < 7) g1_issue_x(c, j - 3, t2, xo_free);
    SCHED();
  }
}

#define EPI_ROUND 0
__device__ __forceinline__ unsigned geglu2(f32x2 g, f32x2 u) {
  const float c1 = -2.0f * 0.7978845608028654f * 1.4426950408889634f;
  const float c2 = c1 * 0.044715f;
  f32x2 p = (g * g) * c2 + c1;
  f32x2 a = g * p;
  f32x2 e; e.x = __builtin_amdgcn_exp2f(a.x); e.y = __builtin_amdgcn_exp2f(a.y);
  f32x2 d = e + 1.0f;
  f32x2 r; r.x = __builtin_amdgcn_rcpf(d.x); r.y = __builtin_amdgcn_rcpf(d.y);
  f32x2 ge = g * r;
  return pack2v(ge * u);
}
__device__ __forceinline__ void st16(unsigned char* p, u32x4 v) {
  asm volatile("global_store_dwordx4 %0, %1, off sc1\n\ts_nop 1" :: "v"(p), "v"(v) : "memory");
}
__device__ __forceinline__ void st8(unsigned char* p, u32x2 v) {
  asm volatile("global_store_dwordx2 %0, %1, off sc1" :: "v"(p), "v"(v) : "memory");
}
__device__ __forceinline__ uint32_t hkb(int kb) { return kb * 8192; }

template <int NP>
__device__ __forceinline__ void g1_store(unsigned char* hb, int nfg0, int i, const u32x2 (&hp)[NP][4]) {
  if (NP == 4) {
    if (i & 1) {
      unsigned char* p = hb + hkb((nfg0 + i) >> 1);
#pragma unroll
      for (int m = 0; m < 4; ++m)
        st16(p + m * 1024, (u32x4){hp[i - 1][m].x, hp[i - 1][m].y, hp[i][m].x, hp[i][m].y});
    }
  } else {
    if (i == 2) {
      if (nfg0 & 1) {
        unsigned char* p = hb + hkb(nfg0 >> 1) + 8;
        unsigned char* q = hb + hkb((nfg0 + 1) >> 1);
#pragma unroll
        for (int m = 0; m < 4; ++m) {
          st8(p + m * 1024, hp[0][m]);
          st16(q + m * 1024, (u32x4){hp[1][m].x, hp[1][m].y, hp[2][m].x, hp[2][m].y});
        }
      } else {
        unsigned char* p = hb + hkb((nfg0 + 2) >> 1);
        unsigned char* q = hb + hkb(nfg0 >> 1);
#pragma unroll
        for (int m = 0; m < 4; ++m) {
          st8(p + m * 1024, hp[2][m]);
          st16(q + m * 1024, (u32x4){hp[0][m].x, hp[0][m].y, hp[1][m].x, hp[1][m].y});
        }
      }
    }
  }
}

template <int NF>
__device__ __forceinline__ void g1_epi(f32x4 (&acc)[NF][4], unsigned char* hb, int nfg0, bool do_store) {
  constexpr int NP = NF / 2;
  u32x2 hp[NP][4];
#pragma unroll
  for (int i = 0; i < NP; ++i) {
#pragma unroll
    for (int e = 0; e < 4; ++e) {
      hp[i][e].x = geglu2((f32x2){acc[2 * i][e][0], acc[2 * i][e][1]}, (f32x2){acc[2 * i + 1][e][0], acc[2 * i + 1][e][1]});
      hp[i][e].y = geglu2((f32x2){acc[2 * i][e][2], acc[2 * i][e][3]}, (f32x2){acc[2 * i + 1][e][2], acc[2 * i + 1][e][3]});
    }
    if (do_store) g1_store<NP>(hb, nfg0, i, hp);
    else {
#pragma unroll
      for (int e = 0; e < 4; ++e) asm volatile("" :: "v"(hp[i][e]));
    }
  }
}

template <int NF>
__device__ __forceinline__ void g1_last(const G1Ctx& c, uint32_t xo_cur, uint32_t wo_cur, f32x4 (&acc)[NF][4], bf16x8 (&xf)[2][4],
                                        bf16x4 (&wlo)[4], bf16x4 (&whi)[4], unsigned char* hb, int nfg0) {
  constexpr int NP = NF / 2;
  constexpr int NS = 2 * NF;
  const uint32_t xa1 = c.xa[1] + xo_cur;
  const uint32_t wac0 = c.wa[0] + wo_cur, wac1 = c.wa[1] + wo_cur;
  u32x2 hp[NP][4];
#pragma unroll
  for (int j = 0; j < NS; ++j) {
    const int i = j >> 2, s = (j >> 1) & 1, f = 2 * i + (j & 1);
    const int j2 = j + 2, i2 = j2 >> 2, s2 = (j2 >> 1) & 1;
    const uint32_t o = s2 * (32 * 224) + (j2 & 1) * 14336 + i2 * 32;
    const bf16x8 w = cat8(wlo[j & 3], whi[j & 3]);
    const int e = j & 3;
    if (j2 < NS) { wlo[j2 & 3] = lds_tr(wac0 + o); whi[j2 & 3] = lds_tr(wac1 + o); }
    if (j < 2) { xf[1][2 * j] = lds_b128(xa1 + (2 * j) * 2048); xf[1][2 * j + 1] = lds_b128(xa1 + (2 * j + 1) * 2048); }
#pragma unroll
    for (int m = 0; m < 4; ++m) acc[f][m] = mfma16(w, xf[s][m], acc[f][m]);
    if (i > 0) {
      hp[i - 1][e].x = geglu2((f32x2){acc[2 * i - 2][e][0], acc[2 * i - 2][e][1]}, (f32x2){acc[2 * i - 1][e][0], acc[2 * i - 1][e][1]});
      hp[i - 1][e].y = geglu2((f32x2){acc[2 * i - 2][e][2], acc[2 * i - 2][e][3]}, (f32x2){acc[2 * i - 1][e][2], acc[2 * i - 1][e][3]});
#pragma unroll
      for (int m = 0; m < 4; ++m) {
        __builtin_amdgcn_sched_group_barrier(0x008, 1, 0);
        __builtin_amdgcn_sched_group_barrier(0x002, 6, 0);
      }
    }
    if (i > 0 && e == 3) g1_store<NP>(hb, nfg0, i - 1, hp);
    SCHED();
  }
#pragma unroll
  for (int e = 0; e < 4; ++e) {
    hp[NP - 1][e].x = geglu2((f32x2){acc[NF - 2][e][0], acc[NF - 2][e][1]}, (f32x2){acc[NF - 1][e][0], acc[NF - 1][e][1]});
    hp[NP - 1][e].y = geglu2((f32x2){acc[NF - 2][e][2], acc[NF - 2][e][3]}, (f32x2){acc[NF - 1][e][2], acc[NF - 1][e][3]});
  }
  g1_store<NP>(hb, nfg0, NP - 1, hp);
}

constexpr size_t WS_XCH = (size_t)32 << 20;
constexpr size_t WS_FLAGS = (size_t)16 << 20;

template <int NF>
__device__ __forceinline__ void g1_run(const G1Ctx& c, unsigned char* hb, unsigned char* xch, int nfg0, int lane) {
  constexpr int NWL = (NF == 8) ? 3 : 4;
  unsigned long long st_wait = 0;
  f32x4 acc[NF][4];
#pragma unroll
  for (int f = 0; f < NF; ++f)
#pragma unroll
    for (int m = 0; m < 4; ++m) acc[f][m] = (f32x4){0.f, 0.f, 0.f, 0.f};
  bf16x8 xf[2][4];
  bf16x4 wlo[4], whi[4];
#pragma unroll
  for (int i = 0; i < 4; ++i) g1_issue_x(c, i, 0, 0);
#pragma unroll
  for (int i = 0; i < NWL; ++i) g1_issue_w(c, i, 0, 0);
#pragma unroll
  for (int i = 0; i < 2; ++i) g1_issue_w(c, i, 1, G1_WB);
  WAIT_VM(2);
  BAR();
#pragma unroll
  for (int m = 0; m < 4; ++m) xf[0][m] = lds_b128(c.xa[0] + m * 2048);
  wlo[0] = lds_tr(c.wa[0]); whi[0] = lds_tr(c.wa[1]);
  wlo[1] = lds_tr(c.wa[0] + 14336); whi[1] = lds_tr(c.wa[1] + 14336);
  SCHED();
#pragma unroll
  for (int i = 0; i < 4; ++i) g1_issue_x(c, i, 1, G1_XB);
  if (c.pfw) {
#pragma unroll
    for (int i = 2; i < G1_PD; ++i) { l2pf(c.pf + (size_t)(64 * F_ * 2) * i, c.scr); l2pf(c.pf2 + (size_t)c.pfs2 * i, c.scr); }
  }
  SCHED();
  uint32_t xo0 = 0, xo1 = G1_XB, xo2 = 2 * G1_XB, wo = 0;
#define LAST_SEP 0
#pragma nounroll
  for (int t = 0; t < G1_NT - LAST_SEP; ++t) {
    g1_tile<NF>(c, t, xo0, xo1, xo2, wo, acc, xf, wlo, whi, st_wait);
    const uint32_t tmp = xo0; xo0 = xo1; xo1 = xo2; xo2 = tmp;
    wo = G1_WB - wo;
  }
  g1_epi<NF>(acc, hb, nfg0, true);
}

#define G2_FRONT 1
constexpr int G2_NT = 28;
constexpr int G2_ST = 32768;
#define G2_R 3
constexpr int G2_RING = G2_R * G2_ST;
constexpr size_t WS_SLOTS = (size_t)17 << 20;

template <int OFF>
__device__ __forceinline__ void glds16_sc1(const void* sbase, uint32_t voff, uint32_t lds_addr) {
  asm volatile("s_mov_b32 m0, %2\n\ts_nop 0\n\tglobal_load_lds_dwordx4 %0, %1 offset:%3 sc1"
               :: "v"(voff), "s"(sbase), "s"(lds_addr), "n"(OFF) : "memory");
}
__device__ __forceinline__ void g2_issue_h(const char* hs, uint32_t hv, int st, uint32_t hdst) {
  const char* hp = hs + (size_t)st * 16384;
  glds16_sc1<0>(hp, hv, hdst);
  glds16_sc1<1024>(hp, hv, hdst);
  glds16_sc1<2048>(hp, hv, hdst);
  glds16_sc1<3072>(hp, hv, hdst);
}
__device__ __forceinline__ void g2_issue_w(const char* ws, uint32_t wv0, uint32_t wv1, int st, uint32_t wdst) {
  const char* wp = ws + (size_t)st * (64 * 2048);
  glds16(wp, wv0, wdst);
  glds16(wp + 4 * 2048, wv1, wdst + 1024);
  glds16(wp + 8 * 2048, wv0, wdst + 2048);
  glds16(wp + 12 * 2048, wv1, wdst + 3072);
}
__device__ __forceinline__ unsigned g2_need(int st) {
  const int lo = (64 * st) / 112, hi = (64 * st + 63) / 112;
  return (1u << lo) | (1u << hi);
}

template <bool RD, bool UPD>
__device__ __forceinline__ void g2_kb(uint32_t (&ha)[2], uint32_t (&wa)[4], uint32_t off, uint32_t delta, f32x4 (&acc)[4][4],
                                      const bf16x8 (&hc)[4], const bf16x4 (&wlc)[4], const bf16x4 (&whc)[4],
                                      bf16x8 (&hn)[4], bf16x4 (&wln)[4], bf16x4 (&whn)[4]) {
#pragma unroll
  for (int f = 0; f < 4; ++f) {
    const bf16x8 w = cat8(wlc[f], whc[f]);
#pragma unroll
    for (int a = 0; a < 4; ++a) {
      acc[f][a] = mfma16(w, hc[a], acc[f][a]);
      SCHED();
      const int g = 4 * f + a;
      if (RD) {
        if (g < 4) hn[g] = lds_b128(ha[g & 1] + off + (g >> 1) * 2048);
        else if (g < 12) { const int q = (g - 4) >> 1; if (g & 1) whn[q] = lds_tr(wa[q] + off + 4096); else wln[q] = lds_tr(wa[q] + off); }
      }
      if (UPD) {
        if (g == 12) { wa[0] += delta; wa[1] += delta; }
        if (g == 13) { wa[2] += delta; wa[3] += delta; }
        if (g == 14) { ha[0] += delta; ha[1] += delta; }
      }
      SCHED();
    }
  }
}

__device__ __forceinline__ void g2_finish(unsigned char* ws_base, int bid, int wid, int lane, f32x4 (&kep)[4], unsigned char* op) {
  const unsigned* iflag = (const unsigned*)(ws_base + WS_FLAGS + (size_t)(bid * 8 + (wid & 3)) * 64);
  for (int spin = 0; spin < (1 << 22); ++spin) {
    const unsigned v = __builtin_amdgcn_readfirstlane(__hip_atomic_load(iflag, __ATOMIC_RELAXED, __HIP_MEMORY_SCOPE_AGENT));
    if (v == 1u) break;
    __builtin_amdgcn_s_sleep(1);
  }
  asm volatile("" ::: "memory");
  const unsigned char* xr = ws_base + WS_XCH + (size_t)(bid * 8 + wid) * 4096 + lane * 16;
  f32x4 o0, o1, o2, o3;
  asm volatile("global_load_dwordx4 %0, %4, off sc1\n\t"
               "global_load_dwordx4 %1, %4, off offset:1024 sc1\n\t"
               "global_load_dwordx4 %2, %4, off offset:2048 sc1\n\t"
               "global_load_dwordx4 %3, %4, off offset:3072 sc1\n\t"
               "s_waitcnt vmcnt(0)"
               : "=&v"(o0), "=&v"(o1), "=&v"(o2), "=&v"(o3) : "v"(xr) : "memory");
  kep[0] += o0; kep[1] += o1; kep[2] += o2; kep[3] += o3;
#pragma unroll
  for (int f = 0; f < 4; ++f) {
    const f32x4 v = kep[f];
    u32x2 pk; pk.x = pack2v((f32x2){v[0], v[1]}); pk.y = pack2v((f32x2){v[2], v[3]});
    *(u32x2*)(op + f * 32) = pk;
  }
}

__global__ __launch_bounds__(512, 2) void ffn_kernel(const unsigned char* __restrict__ x, const unsigned char* __restrict__ wg,
                                                     const unsigned char* __restrict__ wu, const unsigned char* __restrict__ wd,
                                                     unsigned char* __restrict__ out, unsigned char* __restrict__ Hf) {
  __shared__ __attribute__((aligned(1024))) unsigned char smem[G1_LDS];
  const uint32_t lbase = (uint32_t)(uintptr_t)(LAS unsigned char*)smem;
  const int bid = blockIdx.x;
  const int xcd = bid & 7, idx = bid >> 3;
  const int pm = 4 * (xcd & 1) + (idx & 3), pn = 8 * (xcd >> 1) + (idx >> 2);
  const int brow = pm * 256;
  const int lane = threadIdx.x & 63;
  const int wid = __builtin_amdgcn_readfirstlane(threadIdx.x >> 6);
  const int wr = wid & 3, wc = wid >> 2;
  const char* pfa; const char* pfb; uint32_t pfbs;
  {
    const int li = idx * 56 + (lane < 56 ? lane : 0);
    const int mat = li / 896, r = (li % 896) / 14, cc = li % 14;
    pfa = (const char*)(mat ? wu : wg) + (size_t)r * (F_ * 2) + (xcd >> 1) * 1792 + cc * 128;
    const char* px = (const char*)x + (size_t)(brow + 32 * (pn & 7) + (lane & 31)) * 2048;
    const int t0 = wid >> 1;
    if (t0 < EARLY_PF) {
      if (wid & 1) l2pf(pfa + (size_t)(64 * F_ * 2) * t0, lbase + G1_SCR);
      else l2pf(px + 128 * t0, lbase + G1_SCR);
    }
    if (lane >= 32 && lane < 48) {
      pfb = (const char*)wd + (size_t)((bid * 16 + (lane - 32)) * 128) - (size_t)2 * (256 * 2048);
      pfbs = 256 * 2048;
    } else {
      pfb = px;
      pfbs = 128;
    }
  }
  if (wid < CODE_PF_WAVES) {
    const char* pc = (const char*)__builtin_amdgcn_s_getpc();
    l2pf(pc + (size_t)(wid * 64 + lane) * CODE_PF_STRIDE, lbase + G1_SCR);
  }
  unsigned* myslots = (unsigned*)(Hf + WS_SLOTS) + (size_t)(pm * 32 + pn) * 32;
  if (wid == 4) {
    if (lane < 32) __hip_atomic_store(myslots + lane, 0u, __ATOMIC_RELAXED, __HIP_MEMORY_SCOPE_AGENT);
    else if (lane < 40) __hip_atomic_store((unsigned*)(Hf + WS_FLAGS + (size_t)(bid * 8 + lane - 32) * 64), 0u, __ATOMIC_RELAXED, __HIP_MEMORY_SCOPE_AGENT);
  }

  {
    G1Ctx c;
    c.xs = (const char*)x + (size_t)(brow + 32 * wid) * 2048;
#pragma unroll
    for (int a = 0; a < 2; ++a) {
      const int r = 8 * a + (lane >> 3);
      const int ch = (lane & 7) ^ ((4 * a + (lane >> 4)) & 7);
      c.xv[a] = r * 2048 + ch * 16;
    }
    c.xl = lbase + wid * 4096;
    const int e0 = (wid < 4) ? 3 * wid : 12 + 4 * (wid - 4);
#pragma unroll
    for (int j = 0; j < 4; ++j) {
      int e = e0 + j; if (e > 27) e = 27;
      const int mat = e / 14, jj = e % 14;
      const int ci = 64 * jj + lane;
      const int rowp = ci / 14, cc = ci % 14;
      const int row = rowp ^ (((rowp >> 3) & 1) << 2);
      c.wv[j] = row * (F_ * 2) + cc * 16;
      c.ws[j] = (const char*)(mat ? wu : wg) + pn * 224;
      c.wl[j] = lbase + G1_WOFF + mat * 14336 + jj * 1024;
    }
    c.pfw = (wid == 4);
    c.pf = pfa; c.pf2 = pfb; c.pfs2 = pfbs;
    c.scr = lbase + G1_SCR;
    const int g = lane >> 4, r16 = lane & 15;
#pragma unroll
    for (int s = 0; s < 2; ++s)
      c.xa[s] = lbase + (wr * 64 + r16) * 128 + (((4 * s + g) ^ (r16 >> 1)) & 7) * 16;
    const int q = r16 >> 2, p = lane & 3;
    const int fbase = (wc == 0) ? ((pn & 1) ? 3 : 0) : ((pn & 1) ? 0 : 4);
#pragma unroll
    for (int i = 0; i < 2; ++i)
      c.wa[i] = lbase + G1_WOFF + (8 * g + 4 * (i ^ (g & 1)) + q) * 224 + fbase * 32 + p * 8;
    const int nfg0 = 7 * pn + fbase;
    unsigned char* hb = Hf + (size_t)(2 * pm + (wr >> 1)) * (112 * 8192) + (wr & 1) * 4096 + lane * 16;
    if (wc == 0) g1_run<8>(c, hb, nullptr, nfg0, lane);
    else g1_run<6>(c, hb, nullptr, nfg0, lane);
  }
  WAIT_LGKM0();
  WAIT_VM(0);
  BAR();

  const int kh2 = pn >> 4, cp = ((pn >> 3) & 1) * 4 + ((pn >> 1) & 3), rp = 2 * pm + (pn & 1);
#define S0_MASK 15
  const int s0 = ((112 * (pn & S0_MASK) + 63) / 64) % G2_NT;
  const int pbid = bid ^ 4;
  const int wm = wr & 1, wn = wr >> 1;
  unsigned char* op = out + (size_t)(rp * 128 + wm * 64 + wc * 32 + kh2 * 16 + (lane & 15)) * 2048 + cp * 256 + wn * 128 + (lane >> 4) * 8;
  const uint32_t xl = lbase + G2_RING + wr * 4096 + lane * 16;
  if (wc == 1) {
    const int l = wr;
    const char* hs = (const char*)Hf + (size_t)(rp * 112 + kh2 * 56) * 8192 + l * 4096;
    const char* ws = (const char*)wd + (size_t)(kh2 * 1792 + 16 * l) * 2048 + cp * 256;
    const uint32_t hv = lane * 16;
    const int pc = lane & 15, r4 = lane >> 4;
    const uint32_t wv0 = r4 * 2048 + ((((pc >> 1) ^ r4) & 7) * 2 + (pc & 1)) * 16;
    const uint32_t wv1 = r4 * 2048 + ((((pc >> 1) ^ (4 + r4)) & 7) * 2 + (pc & 1)) * 16;
    const uint32_t hb = lbase + l * 4096, wb = lbase + 16384 + l * 4096;
    unsigned* dst = (unsigned*)(Hf + WS_SLOTS) + (size_t)(pm * 32 + (lane & 31)) * 32 + pn;
    unsigned ready = 1u << pn;
#define G2_WAIT_READY(NEED)                                                                                              \
    if (l == 0) {                                                                                                        \
      const unsigned need_ = (NEED);                                                                                     \
      for (int spin = 0; (ready & need_) != need_ && spin < (1 << 20); ++spin) {                                         \
        const unsigned v = __hip_atomic_load(myslots + (lane & 31), __ATOMIC_RELAXED, __HIP_MEMORY_SCOPE_AGENT);         \
        ready |= (unsigned)__ballot(v == 1u);                                                                            \
      }                                                                                                                  \
      asm volatile("" ::: "memory");                                                                                     \
    }
    if (l == 0 && lane < 32) __hip_atomic_store(dst, 1u, __ATOMIC_RELAXED, __HIP_MEMORY_SCOPE_AGENT);
    int sg[G2_R];
#pragma unroll
    for (int i = 0; i < G2_R; ++i) { sg[i] = s0 + i; if (sg[i] >= G2_NT) sg[i] -= G2_NT; }
    g2_issue_h(hs, hv, sg[0], hb);
#pragma unroll
    for (int i = 0; i < G2_R; ++i) g2_issue_w(ws, wv0, wv1, sg[i], wb + i * G2_ST);
    asm volatile("s_waitcnt vmcnt(%0)" :: "n"(4 * (G2_R - 1)) : "memory");
    BAR();
    {
      unsigned need_ = 0;
#pragma unroll
      for (int i = 1; i < G2_R; ++i) need_ |= g2_need(sg[i]);
      need_ <<= 16 * kh2;
      for (int spin = 0; (ready & need_) != need_ && spin < (1 << 20); ++spin) {
        const unsigned v = __hip_atomic_load(myslots + (lane & 31), __ATOMIC_RELAXED, __HIP_MEMORY_SCOPE_AGENT);
        ready |= (unsigned)__ballot(v == 1u);
      }
      asm volatile("" ::: "memory");
    }
#pragma unroll
    for (int i = 1; i < G2_R; ++i) g2_issue_h(hs, hv, sg[i], hb + i * G2_ST);
    int st = sg[G2_R - 1];
    uint32_t so = 0;
    for (int t = 0; t < G2_NT - G2_R; ++t) {
      ++st; if (st >= G2_NT) st -= G2_NT;
      G2_WAIT_READY(g2_need(st) << (16 * kh2));
      if (G2_R == 3) { if (t == 0) WAIT_VM(4); else WAIT_VM(8); }
      else { if (t == 0) WAIT_VM(8); else if (t == 1) WAIT_VM(12); else WAIT_VM(16); }
      BAR();
      g2_issue_h(hs, hv, st, hb + so);
      g2_issue_w(ws, wv0, wv1, st, wb + so);
      so += G2_ST; if (so == G2_RING) so = 0;
    }
    if (G2_R == 4) { WAIT_VM(16); BAR(); }
    WAIT_VM(8);
    BAR();
    WAIT_VM(0);
    BAR();
    G2_WAIT_READY(0xffffffffu);
    if (l == 0 && lane < 32) __hip_atomic_store(dst, 1u, __ATOMIC_RELAXED, __HIP_MEMORY_SCOPE_AGENT);
    BAR();
    f32x4 kep[4];
#pragma unroll
    for (int f = 0; f < 4; ++f) kep[f] = *(LAS f32x4*)(xl + f * 1024);
    g2_finish(Hf, bid, wid, lane, kep, op);
    return;
  }

  uint32_t wa0[4];
  {
    const int g = lane >> 4, q = (lane >> 2) & 3, p = lane & 3;
    const int r = 4 * g + q;
#pragma unroll
    for (int f = 0; f < 4; ++f)
      wa0[f] = lbase + 16384 + r * 256 + (((4 * wn + f) ^ r) & 7) * 32 + p * 8;
  }
  uint32_t ha0[2];
#pragma unroll
  for (int a = 0; a < 2; ++a) ha0[a] = lbase + wm * 4096 + (a ^ kh2) * 1024 + lane * 16;

  f32x4 acc[4][4];
#pragma unroll
  for (int f = 0; f < 4; ++f)
#pragma unroll
    for (int a = 0; a < 4; ++a) acc[f][a] = (f32x4){0.f, 0.f, 0.f, 0.f};
  bf16x8 hf0[4], hf1[4];
  bf16x4 wl0[4], wh0[4], wl1[4], wh1[4];

  BAR();
#pragma unroll
  for (int i = 0; i < 4; ++i) hf0[i] = lds_b128(ha0[i & 1] + (i >> 1) * 2048);
#pragma unroll
  for (int i = 0; i < 4; ++i) { wl0[i] = lds_tr(wa0[i]); wh0[i] = lds_tr(wa0[i] + 4096); }
  int slot = 0;
  for (int t = 0; t < G2_NT - 1; ++t) {
    const uint32_t delta = (slot == G2_R - 1) ? (uint32_t)(-(G2_R - 1) * G2_ST) : (uint32_t)G2_ST;
    slot = (slot == G2_R - 1) ? 0 : slot + 1;
    g2_kb<true, true>(ha0, wa0, 8192, delta, acc, hf0, wl0, wh0, hf1, wl1, wh1);
    WAIT_LGKM0();
    BAR();
    g2_kb<true, false>(ha0, wa0, 0, 0, acc, hf1, wl1, wh1, hf0, wl0, wh0);
  }
  g2_kb<true, false>(ha0, wa0, 8192, 0, acc, hf0, wl0, wh0, hf1, wl1, wh1);
  g2_kb<false, false>(ha0, wa0, 0, 0, acc, hf1, wl1, wh1, hf0, wl0, wh0);
  {
    unsigned char* xs = Hf + WS_XCH + (size_t)(pbid * 8 + wid) * 4096 + lane * 16;
#pragma unroll
    for (int f = 0; f < 4; ++f) {
      st16(xs + f * 1024, __builtin_bit_cast(u32x4, acc[f][1]));
      st16(xs + 16384 + f * 1024, __builtin_bit_cast(u32x4, acc[f][3]));
    }
  }
#pragma unroll
  for (int f = 0; f < 4; ++f) *(LAS f32x4*)(xl + f * 1024) = acc[f][2];
  WAIT_LGKM0();
  BAR();
  WAIT_VM(0);
  if (lane == 0) __hip_atomic_store((unsigned*)(Hf + WS_FLAGS + (size_t)(pbid * 8 + wid) * 64), 1u, __ATOMIC_RELAXED, __HIP_MEMORY_SCOPE_AGENT);
  {
    f32x4 kep[4];
#pragma unroll
    for (int f = 0; f < 4; ++f) kep[f] = acc[f][0];
    g2_finish(Hf, bid, wid, lane, kep, op);
  }
}

extern "C" void kernel_launch(void* const* d_in, const int* in_sizes, int n_in,
                              void* d_out, int out_size, void* d_ws, size_t ws_size,
                              hipStream_t stream) {
  const unsigned char* x = (const unsigned char*)d_in[0];
  const unsigned char* wg = (const unsigned char*)d_in[1];
  const unsigned char* wu = (const unsigned char*)d_in[2];
  const unsigned char* wd = (const unsigned char*)d_in[3];
  ffn_kernel<<<256, 512, 0, stream>>>(x, wg, wu, wd, (unsigned char*)d_out, (unsigned char*)d_ws);
}
```

```cpp
#include <hip/hip_runtime.h>
#include <hip/hip_bf16.h>
#include <stdint.h>

typedef __bf16 bf16x8 __attribute__((ext_vector_type(8)));
typedef __bf16 bf16x4 __attribute__((ext_vector_type(4)));
typedef __bf16 bf16x2 __attribute__((ext_vector_type(2)));
typedef float f32x4 __attribute__((ext_vector_type(4)));
typedef float f32x2 __attribute__((ext_vector_type(2)));
typedef unsigned u32x2 __attribute__((ext_vector_type(2)));
typedef unsigned u32x4 __attribute__((ext_vector_type(4)));
#define LAS __attribute__((address_space(3)))

#define D_ 1024
#define F_ 3584

__device__ __forceinline__ void glds16(const void* sbase, uint32_t voff, uint32_t lds_addr) {
  asm volatile("s_mov_b32 m0, %2\n\ts_nop 0\n\tglobal_load_lds_dwordx4 %0, %1"
               :: "v"(voff), "s"(sbase), "s"(lds_addr) : "memory");
}
template <int OFF>
__device__ __forceinline__ void glds16_sc1(const void* sbase, uint32_t voff, uint32_t lds_addr) {
  asm volatile("s_mov_b32 m0, %2\n\ts_nop 0\n\tglobal_load_lds_dwordx4 %0, %1 offset:%3 sc1"
               :: "v"(voff), "s"(sbase), "s"(lds_addr), "n"(OFF) : "memory");
}
__device__ __forceinline__ void l2pf(const char* addr, uint32_t lds_scratch) {
  asm volatile("s_mov_b32 m0, %1\n\ts_nop 0\n\tglobal_load_lds_dword %0, off"
               :: "v"(addr), "s"(lds_scratch) : "memory");
}
__device__ __forceinline__ void st16(unsigned char* p, u32x4 v) {
  asm volatile("global_store_dwordx4 %0, %1, off sc1\n\ts_nop 1" :: "v"(p), "v"(v) : "memory");
}
__device__ __forceinline__ void st8(unsigned char* p, u32x2 v) {
  asm volatile("global_store_dwordx2 %0, %1, off sc1" :: "v"(p), "v"(v) : "memory");
}
#define WAIT_VM(n) asm volatile("s_waitcnt vmcnt(" #n ")" ::: "memory")
#define WAIT_LGKM0() asm volatile("s_waitcnt lgkmcnt(0)" ::: "memory")
#define BAR() do { asm volatile("" ::: "memory"); __builtin_amdgcn_s_barrier(); asm volatile("" ::: "memory"); } while (0)
#define SCHED() __builtin_amdgcn_sched_barrier(0)

__device__ __forceinline__ bf16x8 lds_b128(uint32_t addr) { return *(LAS bf16x8*)(addr); }
__device__ __forceinline__ bf16x4 lds_tr(uint32_t a) { return __builtin_amdgcn_ds_read_tr16_b64_v4bf16((LAS bf16x4*)(a)); }
__device__ __forceinline__ bf16x8 cat8(bf16x4 lo, bf16x4 hi) { return __builtin_shufflevector(lo, hi, 0, 1, 2, 3, 4, 5, 6, 7); }
__device__ __forceinline__ f32x4 mfma16(bf16x8 a, bf16x8 b, f32x4 c) {
  return __builtin_amdgcn_mfma_f32_16x16x32_bf16(a, b, c, 0, 0, 0);
}
__device__ __forceinline__ unsigned pack2v(f32x2 v) {
  return __builtin_bit_cast(unsigned, __builtin_convertvector(v, bf16x2));
}

constexpr size_t WS_FLAGS = (size_t)16 << 20;
constexpr size_t WS_SLOTS = (size_t)17 << 20;
constexpr size_t WS_XCH = (size_t)32 << 20;

constexpr int G1_XB = 32768;
constexpr int G1_WB = 28672;
constexpr int G1_WOFF = 3 * G1_XB;
constexpr int G1_SCR = 3 * G1_XB + 2 * G1_WB;
constexpr int G1_LDS = G1_SCR + 256;
constexpr int G1_PD = 4;
constexpr int G1_NT = D_ / 64;
constexpr int CODE_PF_WAVES = 3;
constexpr int CODE_PF_STRIDE = 96;

struct G1Ctx {
  const char* xs;
  const char* ws[4];
  uint32_t xv[2];
  uint32_t wv[4];
  uint32_t xl;
  uint32_t wl[4];
  uint32_t xa[2];
  uint32_t wa[2];
  const char* pf;
  const char* pf2;
  uint32_t pfs1, pfs2;
  uint32_t scr;
};

__device__ __forceinline__ void g1_issue_x(const G1Ctx& c, int idx, int t, uint32_t xoff) {
  glds16(c.xs + (idx >> 1) * (16 * 2048) + t * 128, c.xv[idx & 1], c.xl + xoff + idx * 1024);
}
__device__ __forceinline__ void g1_issue_w(const G1Ctx& c, int j, int t, uint32_t woff) {
  glds16(c.ws[j] + (size_t)t * (64 * F_ * 2), c.wv[j], c.wl[j] + woff);
}

template <int NF>
__device__ __forceinline__ void g1_tile(const G1Ctx& c, int t, uint32_t xo_cur, uint32_t xo_nxt, uint32_t xo_free, uint32_t wo_cur,
                                        f32x4 (&acc)[NF][4], bf16x8 (&xf)[2][4], bf16x4 (&wlo)[4], bf16x4 (&whi)[4]) {
  constexpr int NWL = (NF == 8) ? 3 : 4;
  constexpr int NS = 2 * NF;
  const int t1 = (t + 1 < G1_NT) ? t + 1 : G1_NT - 1;
  const int t2 = (t + 2 < G1_NT) ? t + 2 : G1_NT - 1;
  const int tp = (t + G1_PD < G1_NT) ? t + G1_PD : G1_NT - 1;
  const uint32_t wo_nxt = G1_WB - wo_cur;
  const uint32_t xa1 = c.xa[1] + xo_cur;
  const uint32_t xa0n = c.xa[0] + xo_nxt;
  const uint32_t wac0 = c.wa[0] + wo_cur, wac1 = c.wa[1] + wo_cur;
  const uint32_t wan0 = c.wa[0] + wo_nxt, wan1 = c.wa[1] + wo_nxt;
#pragma unroll
  for (int j = 0; j < NS; ++j) {
    const int s = j / NF, f = j % NF;
    if (j == NS - 2) {
      WAIT_LGKM0();
      WAIT_VM(6);
      BAR();
    }
    uint32_t a0, a1;
    if (j + 2 < NS) {
      const int s2 = (j + 2) / NF, f2 = (j + 2) % NF;
      const uint32_t o = s2 * (32 * 224) + (f2 & 1) * 14336 + (f2 >> 1) * 32;
      a0 = wac0 + o; a1 = wac1 + o;
    } else {
      const int f2 = j + 2 - NS;
      const uint32_t o = (f2 & 1) * 14336 + (f2 >> 1) * 32;
      a0 = wan0 + o; a1 = wan1 + o;
    }
    const bf16x8 w = cat8(wlo[j & 3], whi[j & 3]);
    acc[f][0] = mfma16(w, xf[s][0], acc[f][0]);
    SCHED();
    wlo[(j + 2) & 3] = lds_tr(a0);
    SCHED();
    acc[f][1] = mfma16(w, xf[s][1], acc[f][1]);
    SCHED();
    whi[(j + 2) & 3] = lds_tr(a1);
    SCHED();
    acc[f][2] = mfma16(w, xf[s][2], acc[f][2]);
    SCHED();
    if (j < 4) xf[1][j] = lds_b128(xa1 + j * 2048);
    if (j >= NS - 2) {
      const int m0 = 2 * (j - (NS - 2));
      xf[0][m0] = lds_b128(xa0n + m0 * 2048);
      xf[0][m0 + 1] = lds_b128(xa0n + (m0 + 1) * 2048);
    }
    SCHED();
    acc[f][3] = mfma16(w, xf[s][3], acc[f][3]);
    SCHED();
    if (j >= NS - 2) g1_issue_w(c, j - (NS - 2), t2, wo_cur);
    if (j + 2 < NWL) g1_issue_w(c, j + 2, t1, wo_nxt);
    if (j == 2) { l2pf(c.pf + (size_t)c.pfs1 * tp, c.scr); l2pf(c.pf2 + (size_t)c.pfs2 * tp, c.scr); }
    if (j >= 3 && j < 7) g1_issue_x(c, j - 3, t2, xo_free);
    SCHED();
  }
}

__device__ __forceinline__ unsigned geglu2(f32x2 g, f32x2 u) {
  const float c1 = -2.0f * 0.7978845608028654f * 1.4426950408889634f;
  const float c2 = c1 * 0.044715f;
  f32x2 p = (g * g) * c2 + c1;
  f32x2 a = g * p;
  f32x2 e; e.x = __builtin_amdgcn_exp2f(a.x); e.y = __builtin_amdgcn_exp2f(a.y);
  f32x2 d = e + 1.0f;
  f32x2 r; r.x = __builtin_amdgcn_rcpf(d.x); r.y = __builtin_amdgcn_rcpf(d.y);
  f32x2 ge = g * r;
  return pack2v(ge * u);
}

template <int NF>
__device__ __forceinline__ void g1_epi(f32x4 (&acc)[NF][4], unsigned char* hb, int nfg0) {
  constexpr int NP = NF / 2;
  u32x2 hp[NP][4];
#pragma unroll
  for (int i = 0; i < NP; ++i) {
#pragma unroll
    for (int e = 0; e < 4; ++e) {
      hp[i][e].x = geglu2((f32x2){acc[2 * i][e][0], acc[2 * i][e][1]}, (f32x2){acc[2 * i + 1][e][0], acc[2 * i + 1][e][1]});
      hp[i][e].y = geglu2((f32x2){acc[2 * i][e][2], acc[2 * i][e][3]}, (f32x2){acc[2 * i + 1][e][2], acc[2 * i + 1][e][3]});
    }
    if (NP == 4) {
      if (i & 1) {
        unsigned char* p = hb + ((nfg0 + i) >> 1) * 8192;
#pragma unroll
        for (int m = 0; m < 4; ++m)
          st16(p + m * 1024, (u32x4){hp[i - 1][m].x, hp[i - 1][m].y, hp[i][m].x, hp[i][m].y});
      }
    } else if (i == 2) {
      if (nfg0 & 1) {
        unsigned char* p = hb + (nfg0 >> 1) * 8192 + 8;
        unsigned char* q = hb + ((nfg0 + 1) >> 1) * 8192;
#pragma unroll
        for (int m = 0; m < 4; ++m) {
          st8(p + m * 1024, hp[0][m]);
          st16(q + m * 1024, (u32x4){hp[1][m].x, hp[1][m].y, hp[2][m].x, hp[2][m].y});
        }
      } else {
        unsigned char* p = hb + ((nfg0 + 2) >> 1) * 8192;
        unsigned char* q = hb + (nfg0 >> 1) * 8192;
#pragma unroll
        for (int m = 0; m < 4; ++m) {
          st8(p + m * 1024, hp[2][m]);
          st16(q + m * 1024, (u32x4){hp[0][m].x, hp[0][m].y, hp[1][m].x, hp[1][m].y});
        }
      }
    }
  }
}

template <int NF>
__device__ __forceinline__ void g1_run(const G1Ctx& c, unsigned char* hb, int nfg0) {
  constexpr int NWL = (NF == 8) ? 3 : 4;
  f32x4 acc[NF][4];
#pragma unroll
  for (int f = 0; f < NF; ++f)
#pragma unroll
    for (int m = 0; m < 4; ++m) acc[f][m] = (f32x4){0.f, 0.f, 0.f, 0.f};
  bf16x8 xf[2][4];
  bf16x4 wlo[4], whi[4];
#pragma unroll
  for (int i = 0; i < 4; ++i) g1_issue_x(c, i, 0, 0);
#pragma unroll
  for (int i = 0; i < NWL; ++i) g1_issue_w(c, i, 0, 0);
#pragma unroll
  for (int i = 0; i < 2; ++i) g1_issue_w(c, i, 1, G1_WB);
  WAIT_VM(2);
  BAR();
#pragma unroll
  for (int m = 0; m < 4; ++m) xf[0][m] = lds_b128(c.xa[0] + m * 2048);
  wlo[0] = lds_tr(c.wa[0]); whi[0] = lds_tr(c.wa[1]);
  wlo[1] = lds_tr(c.wa[0] + 14336); whi[1] = lds_tr(c.wa[1] + 14336);
  SCHED();
#pragma unroll
  for (int i = 0; i < 4; ++i) g1_issue_x(c, i, 1, G1_XB);
  if (c.pfs1 != 0) {
#pragma unroll
    for (int i = 2; i < G1_PD; ++i) { l2pf(c.pf + (size_t)c.pfs1 * i, c.scr); l2pf(c.pf2 + (size_t)c.pfs2 * i, c.scr); }
  }
  SCHED();
  uint32_t xo0 = 0, xo1 = G1_XB, xo2 = 2 * G1_XB, wo = 0;
#pragma nounroll
  for (int t = 0; t < G1_NT; ++t) {
    g1_tile<NF>(c, t, xo0, xo1, xo2, wo, acc, xf, wlo, whi);
    const uint32_t tmp = xo0; xo0 = xo1; xo1 = xo2; xo2 = tmp;
    wo = G1_WB - wo;
  }
  g1_epi<NF>(acc, hb, nfg0);
}

constexpr int G2_NT = 28;
constexpr int G2_ST = 32768;
constexpr int G2_RING = 3 * G2_ST;

__device__ __forceinline__ void g2_issue_h(const char* hs, uint32_t hv, int st, uint32_t hdst) {
  const char* hp = hs + (size_t)st * 16384;
  glds16_sc1<0>(hp, hv, hdst);
  glds16_sc1<1024>(hp, hv, hdst);
  glds16_sc1<2048>(hp, hv, hdst);
  glds16_sc1<3072>(hp, hv, hdst);
}
__device__ __forceinline__ void g2_issue_w(const char* ws, uint32_t wv0, uint32_t wv1, int st, uint32_t wdst) {
  const char* wp = ws + (size_t)st * (64 * 2048);
  glds16(wp, wv0, wdst);
  glds16(wp + 4 * 2048, wv1, wdst + 1024);
  glds16(wp + 8 * 2048, wv0, wdst + 2048);
  glds16(wp + 12 * 2048, wv1, wdst + 3072);
}
__device__ __forceinline__ unsigned g2_need(int st) {
  const int lo = (64 * st) / 112, hi = (64 * st + 63) / 112;
  return (1u << lo) | (1u << hi);
}
__device__ __forceinline__ void g2_poll(const unsigned* myslots, int lane, unsigned need, unsigned& ready) {
  for (int spin = 0; (ready & need) != need && spin < (1 << 20); ++spin) {
    const unsigned v = __hip_atomic_load(myslots + (lane & 31), __ATOMIC_RELAXED, __HIP_MEMORY_SCOPE_AGENT);
    ready |= (unsigned)__ballot(v == 1u);
  }
  asm volatile("" ::: "memory");
}

template <bool RD, bool UPD>
__device__ __forceinline__ void g2_kb(uint32_t (&ha)[2], uint32_t (&wa)[4], uint32_t off, uint32_t delta, f32x4 (&acc)[4][4],
                                      const bf16x8 (&hc)[4], const bf16x4 (&wlc)[4], const bf16x4 (&whc)[4],
                                      bf16x8 (&hn)[4], bf16x4 (&wln)[4], bf16x4 (&whn)[4]) {
#pragma unroll
  for (int f = 0; f < 4; ++f) {
    const bf16x8 w = cat8(wlc[f], whc[f]);
#pragma unroll
    for (int a = 0; a < 4; ++a) {
      acc[f][a] = mfma16(w, hc[a], acc[f][a]);
      SCHED();
      const int g = 4 * f + a;
      if (RD) {
        if (g < 4) hn[g] = lds_b128(ha[g & 1] + off + (g >> 1) * 2048);
        else if (g < 12) { const int q = (g - 4) >> 1; if (g & 1) whn[q] = lds_tr(wa[q] + off + 4096); else wln[q] = lds_tr(wa[q] + off); }
      }
      if (UPD) {
        if (g == 12) { wa[0] += delta; wa[1] += delta; }
        if (g == 13) { wa[2] += delta; wa[3] += delta; }
        if (g == 14) { ha[0] += delta; ha[1] += delta; }
      }
      SCHED();
    }
  }
}

__device__ __forceinline__ void g2_finish(unsigned char* ws_base, int bid, int wid, int lane, f32x4 (&kep)[4], unsigned char* op) {
  const unsigned* iflag = (const unsigned*)(ws_base + WS_FLAGS + (size_t)(bid * 4 + (wid & 3)) * 64);
  for (int spin = 0; spin < (1 << 22); ++spin) {
    const unsigned v = __builtin_amdgcn_readfirstlane(__hip_atomic_load(iflag, __ATOMIC_RELAXED, __HIP_MEMORY_SCOPE_AGENT));
    if (v == 1u) break;
    __builtin_amdgcn_s_sleep(1);
  }
  asm volatile("" ::: "memory");
  const unsigned char* xr = ws_base + WS_XCH + (size_t)(bid * 8 + wid) * 4096 + lane * 16;
  f32x4 o0, o1, o2, o3;
  asm volatile("global_load_dwordx4 %0, %4, off sc1\n\t"
               "global_load_dwordx4 %1, %4, off offset:1024 sc1\n\t"
               "global_load_dwordx4 %2, %4, off offset:2048 sc1\n\t"
               "global_load_dwordx4 %3, %4, off offset:3072 sc1\n\t"
               "s_waitcnt vmcnt(0)"
               : "=&v"(o0), "=&v"(o1), "=&v"(o2), "=&v"(o3) : "v"(xr) : "memory");
  kep[0] += o0; kep[1] += o1; kep[2] += o2; kep[3] += o3;
#pragma unroll
  for (int f = 0; f < 4; ++f) {
    const f32x4 v = kep[f];
    u32x2 pk; pk.x = pack2v((f32x2){v[0], v[1]}); pk.y = pack2v((f32x2){v[2], v[3]});
    *(u32x2*)(op + f * 32) = pk;
  }
}

__global__ __launch_bounds__(512, 2) void ffn_kernel(const unsigned char* __restrict__ x, const unsigned char* __restrict__ wg,
                                                     const unsigned char* __restrict__ wu, const unsigned char* __restrict__ wd,
                                                     unsigned char* __restrict__ out, unsigned char* __restrict__ Hf) {
  __shared__ __attribute__((aligned(1024))) unsigned char smem[G1_LDS];
  const uint32_t lbase = (uint32_t)(uintptr_t)(LAS unsigned char*)smem;
  const int bid = blockIdx.x;
  const int xcd = bid & 7, idx = bid >> 3;
  const int pm = 4 * (xcd & 1) + (idx & 3), pn = 8 * (xcd >> 1) + (idx >> 2);
  const int brow = pm * 256;
  const int lane = threadIdx.x & 63;
  const int wid = __builtin_amdgcn_readfirstlane(threadIdx.x >> 6);
  const int wr = wid & 3, wc = wid >> 2;

  const char* pfa; const char* pfb; uint32_t pfbs;
  {
    const int li = idx * 56 + (lane < 56 ? lane : 0);
    const int mat = li / 896, r = (li % 896) / 14, cc = li % 14;
    pfa = (const char*)(mat ? wu : wg) + (size_t)r * (F_ * 2) + (xcd >> 1) * 1792 + cc * 128;
    const char* px = (const char*)x + (size_t)(brow + 32 * (pn & 7) + (lane & 31)) * 2048;
    if (wid < 4) {
      if (wid & 1) l2pf(pfa + (size_t)(64 * F_ * 2) * (wid >> 1), lbase + G1_SCR);
      else l2pf(px + 128 * (wid >> 1), lbase + G1_SCR);
    }
    if (lane >= 32 && lane < 48) {
      pfb = (const char*)wd + (size_t)((bid * 16 + (lane - 32)) * 128) - (size_t)2 * (256 * 2048);
      pfbs = 256 * 2048;
    } else {
      pfb = px;
      pfbs = 128;
    }
  }
  if (wid < CODE_PF_WAVES) {
    const char* pc = (const char*)__builtin_amdgcn_s_getpc();
    l2pf(pc + (size_t)(wid * 64 + lane) * CODE_PF_STRIDE, lbase + G1_SCR);
  }
  unsigned* myslots = (unsigned*)(Hf + WS_SLOTS) + (size_t)(pm * 32 + pn) * 32;
  if (wid == 4) {
    if (lane < 32) __hip_atomic_store(myslots + lane, 0u, __ATOMIC_RELAXED, __HIP_MEMORY_SCOPE_AGENT);
    else if (lane < 36) __hip_atomic_store((unsigned*)(Hf + WS_FLAGS + (size_t)(bid * 4 + lane - 32) * 64), 0u, __ATOMIC_RELAXED, __HIP_MEMORY_SCOPE_AGENT);
  }

  {
    G1Ctx c;
    c.xs = (const char*)x + (size_t)(brow + 32 * wid) * 2048;
#pragma unroll
    for (int a = 0; a < 2; ++a) {
      const int r = 8 * a + (lane >> 3);
      const int ch = (lane & 7) ^ ((4 * a + (lane >> 4)) & 7);
      c.xv[a] = r * 2048 + ch * 16;
    }
    c.xl = lbase + wid * 4096;
    const int e0 = (wid < 4) ? 3 * wid : 12 + 4 * (wid - 4);
#pragma unroll
    for (int j = 0; j < 4; ++j) {
      int e = e0 + j; if (e > 27) e = 27;
      const int mat = e / 14, jj = e % 14;
      const int ci = 64 * jj + lane;
      const int rowp = ci / 14, cc = ci % 14;
      const int row = rowp ^ (((rowp >> 3) & 1) << 2);
      c.wv[j] = row * (F_ * 2) + cc * 16;
      c.ws[j] = (const char*)(mat ? wu : wg) + pn * 224;
      c.wl[j] = lbase + G1_WOFF + mat * 14336 + jj * 1024;
    }
    c.pf = pfa; c.pf2 = pfb; c.pfs1 = 64 * F_ * 2; c.pfs2 = pfbs;
    if (wid != 4) { c.pf = (const char*)x; c.pf2 = (const char*)x; c.pfs1 = 0; c.pfs2 = 0; }
    c.scr = lbase + G1_SCR;
    const int g = lane >> 4, r16 = lane & 15;
#pragma unroll
    for (int s = 0; s < 2; ++s)
      c.xa[s] = lbase + (wr * 64 + r16) * 128 + (((4 * s + g) ^ (r16 >> 1)) & 7) * 16;
    const int q = r16 >> 2, p = lane & 3;
    const int fbase = (wc == 0) ? ((pn & 1) ? 3 : 0) : ((pn & 1) ? 0 : 4);
#pragma unroll
    for (int i = 0; i < 2; ++i)
      c.wa[i] = lbase + G1_WOFF + (8 * g + 4 * (i ^ (g & 1)) + q) * 224 + fbase * 32 + p * 8;
    const int nfg0 = 7 * pn + fbase;
    unsigned char* hb = Hf + (size_t)(2 * pm + (wr >> 1)) * (112 * 8192) + (wr & 1) * 4096 + lane * 16;
    if (wc == 0) g1_run<8>(c, hb, nfg0);
    else g1_run<6>(c, hb, nfg0);
  }
  WAIT_LGKM0();
  WAIT_VM(0);
  BAR();

  const int kh2 = pn >> 4, cp = ((pn >> 3) & 1) * 4 + ((pn >> 1) & 3), rp = 2 * pm + (pn & 1);
  const int s0 = ((112 * (pn & 15) + 63) / 64) % G2_NT;
  const int pbid = bid ^ 4;
  const int wm = wr & 1, wn = wr >> 1;
  unsigned char* op = out + (size_t)(rp * 128 + wm * 64 + wc * 32 + kh2 * 16 + (lane & 15)) * 2048 + cp * 256 + wn * 128 + (lane >> 4) * 8;
  const uint32_t xl = lbase + G2_RING + wr * 4096 + lane * 16;

  if (wc == 1) {
    const int l = wr;
    const char* hs = (const char*)Hf + (size_t)(rp * 112 + kh2 * 56) * 8192 + l * 4096;
    const char* ws = (const char*)wd + (size_t)(kh2 * 1792 + 16 * l) * 2048 + cp * 256;
    const uint32_t hv = lane * 16;
    const int pc = lane & 15, r4 = lane >> 4;
    const uint32_t wv0 = r4 * 2048 + ((((pc >> 1) ^ r4) & 7) * 2 + (pc & 1)) * 16;
    const uint32_t wv1 = r4 * 2048 + ((((pc >> 1) ^ (4 + r4)) & 7) * 2 + (pc & 1)) * 16;
    const uint32_t hb = lbase + l * 4096, wb = lbase + 16384 + l * 4096;
    unsigned* dst = (unsigned*)(Hf + WS_SLOTS) + (size_t)(pm * 32 + (lane & 31)) * 32 + pn;
    unsigned ready = 1u << pn;
    if (l == 0 && lane < 32) __hip_atomic_store(dst, 1u, __ATOMIC_RELAXED, __HIP_MEMORY_SCOPE_AGENT);
    int sb = s0 + 1; if (sb >= G2_NT) sb -= G2_NT;
    int sc = sb + 1; if (sc >= G2_NT) sc -= G2_NT;
    g2_issue_h(hs, hv, s0, hb);
    g2_issue_w(ws, wv0, wv1, s0, wb);
    g2_issue_w(ws, wv0, wv1, sb, wb + G2_ST);
    g2_issue_w(ws, wv0, wv1, sc, wb + 2 * G2_ST);
    WAIT_VM(8);
    BAR();
    g2_poll(myslots, lane, (g2_need(sb) | g2_need(sc)) << (16 * kh2), ready);
    g2_issue_h(hs, hv, sb, hb + G2_ST);
    g2_issue_h(hs, hv, sc, hb + 2 * G2_ST);
    int st = sc;
    uint32_t so = 0;
    for (int t = 0; t < G2_NT - 3; ++t) {
      ++st; if (st >= G2_NT) st -= G2_NT;
      if (l == 0) g2_poll(myslots, lane, g2_need(st) << (16 * kh2), ready);
      if (t == 0) WAIT_VM(4); else WAIT_VM(8);
      BAR();
      g2_issue_h(hs, hv, st, hb + so);
      g2_issue_w(ws, wv0, wv1, st, wb + so);
      so += G2_ST; if (so == G2_RING) so = 0;
    }
    WAIT_VM(8);
    BAR();
    WAIT_VM(0);
    BAR();
    if (l == 0) {
      g2_poll(myslots, lane, 0xffffffffu, ready);
      if (lane < 32) __hip_atomic_store(dst, 1u, __ATOMIC_RELAXED, __HIP_MEMORY_SCOPE_AGENT);
    }
    BAR();
    f32x4 kep[4];
#pragma unroll
    for (int f = 0; f < 4; ++f) kep[f] = *(LAS f32x4*)(xl + f * 1024);
    g2_finish(Hf, bid, wid, lane, kep, op);
    return;
  }

  uint32_t wa0[4];
  {
    const int g = lane >> 4, q = (lane >> 2) & 3, p = lane & 3;
    const int r = 4 * g + q;
#pragma unroll
    for (int f = 0; f < 4; ++f)
      wa0[f] = lbase + 16384 + r * 256 + (((4 * wn + f) ^ r) & 7) * 32 + p * 8;
  }
  uint32_t ha0[2];
#pragma unroll
  for (int a = 0; a < 2; ++a) ha0[a] = lbase + wm * 4096 + (a ^ kh2) * 1024 + lane * 16;

  f32x4 acc[4][4];
#pragma unroll
  for (int f = 0; f < 4; ++f)
#pragma unroll
    for (int a = 0; a < 4; ++a) acc[f][a] = (f32x4){0.f, 0.f, 0.f, 0.f};
  bf16x8 hf0[4], hf1[4];
  bf16x4 wl0[4], wh0[4], wl1[4], wh1[4];

  BAR();
#pragma unroll
  for (int i = 0; i < 4; ++i) hf0[i] = lds_b128(ha0[i & 1] + (i >> 1) * 2048);
#pragma unroll
  for (int i = 0; i < 4; ++i) { wl0[i] = lds_tr(wa0[i]); wh0[i] = lds_tr(wa0[i] + 4096); }
  int slot = 0;
  for (int t = 0; t < G2_NT - 1; ++t) {
    const uint32_t delta = (slot == 2) ? (uint32_t)(-2 * G2_ST) : (uint32_t)G2_ST;
    slot = (slot == 2) ? 0 : slot + 1;
    g2_kb<true, true>(ha0, wa0, 8192, delta, acc, hf0, wl0, wh0, hf1, wl1, wh1);
    WAIT_LGKM0();
    BAR();
    g2_kb<true, false>(ha0, wa0, 0, 0, acc, hf1, wl1, wh1, hf0, wl0, wh0);
  }
  g2_kb<true, false>(ha0, wa0, 8192, 0, acc, hf0, wl0, wh0, hf1, wl1, wh1);
  g2_kb<false, false>(ha0, wa0, 0, 0, acc, hf1, wl1, wh1, hf0, wl0, wh0);
  SCHED();
  asm volatile("s_nop 15" ::: "memory");
  SCHED();
  {
    unsigned char* xs = Hf + WS_XCH + (size_t)(pbid * 8 + wid) * 4096 + lane * 16;
#pragma unroll
    for (int f = 0; f < 4; ++f) {
      st16(xs + f * 1024, __builtin_bit_cast(u32x4, acc[f][1]));
      st16(xs + 16384 + f * 1024, __builtin_bit_cast(u32x4, acc[f][3]));
    }
  }
#pragma unroll
  for (int f = 0; f < 4; ++f) *(LAS f32x4*)(xl + f * 1024) = acc[f][2];
  WAIT_LGKM0();
  BAR();
  WAIT_VM(0);
  if (lane == 0) __hip_atomic_store((unsigned*)(Hf + WS_FLAGS + (size_t)(pbid * 4 + wid) * 64), 1u, __ATOMIC_RELAXED, __HIP_MEMORY_SCOPE_AGENT);
  {
    f32x4 kep[4];
#pragma unroll
    for (int f = 0; f < 4; ++f) kep[f] = acc[f][0];
    g2_finish(Hf, bid, wid, lane, kep, op);
  }
}

extern "C" void kernel_launch(void* const* d_in, const int* in_sizes, int n_in,
                              void* d_out, int out_size, void* d_ws, size_t ws_size,
                              hipStream_t stream) {
  const unsigned char* x = (const unsigned char*)d_in[0];
  const unsigned char* wg = (const unsigned char*)d_in[1];
  const unsigned char* wu = (const unsigned char*)d_in[2];
  const unsigned char* wd = (const unsigned char*)d_in[3];
  ffn_kernel<<<256, 512, 0, stream>>>(x, wg, wu, wd, (unsigned char*)d_out, (unsigned char*)d_ws);
}
```

```cpp
#include <hip/hip_runtime.h>
#include <hip/hip_bf16.h>
#include <stdint.h>

typedef __bf16 bf16x8 __attribute__((ext_vector_type(8)));
typedef __bf16 bf16x4 __attribute__((ext_vector_type(4)));
typedef __bf16 bf16x2 __attribute__((ext_vector_type(2)));
typedef float f32x4 __attribute__((ext_vector_type(4)));
typedef float f32x2 __attribute__((ext_vector_type(2)));
typedef unsigned u32x2 __attribute__((ext_vector_type(2)));
typedef unsigned u32x4 __attribute__((ext_vector_type(4)));
#define LAS __attribute__((address_space(3)))

#define D_ 1024
#define F_ 3584

__device__ __forceinline__ void glds16(const void* sbase, uint32_t voff, uint32_t lds_addr) {
  asm volatile("s_mov_b32 m0, %2\n\ts_nop 0\n\tglobal_load_lds_dwordx4 %0, %1"
               :: "v"(voff), "s"(sbase), "s"(lds_addr) : "memory");
}
template <int OFF>
__device__ __forceinline__ void glds16_sc1(const void* sbase, uint32_t voff, uint32_t lds_addr) {
  asm volatile("s_mov_b32 m0, %2\n\ts_nop 0\n\tglobal_load_lds_dwordx4 %0, %1 offset:%3 sc1"
               :: "v"(voff), "s"(sbase), "s"(lds_addr), "n"(OFF) : "memory");
}
__device__ __forceinline__ void l2pf(const char* addr, uint32_t lds_scratch) {
  asm volatile("s_mov_b32 m0, %1\n\ts_nop 0\n\tglobal_load_lds_dword %0, off"
               :: "v"(addr), "s"(lds_scratch) : "memory");
}
__device__ __forceinline__ void st16(unsigned char* p, u32x4 v) {
  asm volatile("global_store_dwordx4 %0, %1, off sc1\n\ts_nop 1" :: "v"(p), "v"(v) : "memory");
}
__device__ __forceinline__ void st8(unsigned char* p, u32x2 v) {
  asm volatile("global_store_dwordx2 %0, %1, off sc1" :: "v"(p), "v"(v) : "memory");
}
#define WAIT_VM(n) asm volatile("s_waitcnt vmcnt(" #n ")" ::: "memory")
#define WAIT_LGKM0() asm volatile("s_waitcnt lgkmcnt(0)" ::: "memory")
#define BAR() do { asm volatile("" ::: "memory"); __builtin_amdgcn_s_barrier(); asm volatile("" ::: "memory"); } while (0)
#define SCHED() __builtin_amdgcn_sched_barrier(0)

__device__ __forceinline__ bf16x8 lds_b128(uint32_t addr) { return *(LAS bf16x8*)(addr); }
__device__ __forceinline__ bf16x4 lds_tr(uint32_t a) { return __builtin_amdgcn_ds_read_tr16_b64_v4bf16((LAS bf16x4*)(a)); }
__device__ __forceinline__ bf16x8 cat8(bf16x4 lo, bf16x4 hi) { return __builtin_shufflevector(lo, hi, 0, 1, 2, 3, 4, 5, 6, 7); }
__device__ __forceinline__ f32x4 mfma16(bf16x8 a, bf16x8 b, f32x4 c) {
  return __builtin_amdgcn_mfma_f32_16x16x32_bf16(a, b, c, 0, 0, 0);
}
__device__ __forceinline__ unsigned pack2v(f32x2 v) {
  return __builtin_bit_cast(unsigned, __builtin_convertvector(v, bf16x2));
}

constexpr size_t WS_FLAGS = (size_t)16 << 20;
constexpr size_t WS_SLOTS = (size_t)17 << 20;
constexpr size_t WS_XCH = (size_t)32 << 20;

constexpr int G1_XB = 32768;
constexpr int G1_WB = 28672;
constexpr int G1_WOFF = 3 * G1_XB;
constexpr int G1_SCR = 3 * G1_XB + 2 * G1_WB;
constexpr int G1_LDS = G1_SCR + 256;
constexpr int G1_PD = 4;
constexpr int G1_NT = D_ / 64;
constexpr int CODE_PF_STRIDE = 100;

struct G1Ctx {
  const char* xs;
  const char* ws[4];
  uint32_t xv[2];
  uint32_t wv[4];
  uint32_t xl;
  uint32_t wl[4];
  uint32_t xa[2];
  uint32_t wa[2];
  const char* pf;
  const char* pf2;
  uint32_t pfs1, pfs2;
  uint32_t scr;
};

__device__ __forceinline__ void g1_issue_x(const G1Ctx& c, int idx, int t, uint32_t xoff) {
  glds16(c.xs + (idx >> 1) * (16 * 2048) + t * 128, c.xv[idx & 1], c.xl + xoff + idx * 1024);
}
__device__ __forceinline__ void g1_issue_w(const G1Ctx& c, int j, int t, uint32_t woff) {
  glds16(c.ws[j] + (size_t)t * (64 * F_ * 2), c.wv[j], c.wl[j] + woff);
}

template <int NF>
__device__ __forceinline__ void g1_tile(const G1Ctx& c, int t, uint32_t xo_cur, uint32_t xo_nxt, uint32_t xo_free, uint32_t wo_cur,
                                        f32x4 (&acc)[NF][4], bf16x8 (&xf)[2][4], bf16x4 (&wlo)[4], bf16x4 (&whi)[4]) {
  constexpr int NWL = (NF == 8) ? 3 : 4;
  constexpr int NS = 2 * NF;
  const int t1 = (t + 1 < G1_NT) ? t + 1 : G1_NT - 1;
  const int t2 = (t + 2 < G1_NT) ? t + 2 : G1_NT - 1;
  const int tp = (t + G1_PD < G1_NT) ? t + G1_PD : G1_NT - 1;
  const uint32_t wo_nxt = G1_WB - wo_cur;
  const uint32_t xa1 = c.xa[1] + xo_cur;
  const uint32_t xa0n = c.xa[0] + xo_nxt;
  const uint32_t wac0 = c.wa[0] + wo_cur, wac1 = c.wa[1] + wo_cur;
  const uint32_t wan0 = c.wa[0] + wo_nxt, wan1 = c.wa[1] + wo_nxt;
#pragma unroll
  for (int j = 0; j < NS; ++j) {
    const int s = j / NF, f = j % NF;
    if (j == NS - 2) {
      WAIT_LGKM0();
      WAIT_VM(6);
      BAR();
    }
    uint32_t a0, a1;
    if (j + 2 < NS) {
      const int s2 = (j + 2) / NF, f2 = (j + 2) % NF;
      const uint32_t o = s2 * (32 * 224) + (f2 & 1) * 14336 + (f2 >> 1) * 32;
      a0 = wac0 + o; a1 = wac1 + o;
    } else {
      const int f2 = j + 2 - NS;
      const uint32_t o = (f2 & 1) * 14336 + (f2 >> 1) * 32;
      a0 = wan0 + o; a1 = wan1 + o;
    }
    const bf16x8 w = cat8(wlo[j & 3], whi[j & 3]);
    acc[f][0] = mfma16(w, xf[s][0], acc[f][0]);
    SCHED();
    wlo[(j + 2) & 3] = lds_tr(a0);
    SCHED();
    acc[f][1] = mfma16(w, xf[s][1], acc[f][1]);
    SCHED();
    whi[(j + 2) & 3] = lds_tr(a1);
    SCHED();
    acc[f][2] = mfma16(w, xf[s][2], acc[f][2]);
    SCHED();
    if (j < 4) xf[1][j] = lds_b128(xa1 + j * 2048);
    if (j >= NS - 2) {
      const int m0 = 2 * (j - (NS - 2));
      xf[0][m0] = lds_b128(xa0n + m0 * 2048);
      xf[0][m0 + 1] = lds_b128(xa0n + (m0 + 1) * 2048);
    }
    SCHED();
    acc[f][3] = mfma16(w, xf[s][3], acc[f][3]);
    SCHED();
    if (j >= NS - 2) g1_issue_w(c, j - (NS - 2), t2, wo_cur);
    if (j + 2 < NWL) g1_issue_w(c, j + 2, t1, wo_nxt);
    if (j == 2) { l2pf(c.pf + (size_t)c.pfs1 * tp, c.scr); l2pf(c.pf2 + (size_t)c.pfs2 * tp, c.scr); }
    if (j >= 3 && j < 7) g1_issue_x(c, j - 3, t2, xo_free);
    SCHED();
  }
}

__device__ __forceinline__ unsigned geglu2(f32x2 g, f32x2 u) {
  const float c1 = -2.0f * 0.7978845608028654f * 1.4426950408889634f;
  const float c2 = c1 * 0.044715f;
  f32x2 p = (g * g) * c2 + c1;
  f32x2 a = g * p;
  f32x2 e; e.x = __builtin_amdgcn_exp2f(a.x); e.y = __builtin_amdgcn_exp2f(a.y);
  f32x2 d = e + 1.0f;
  f32x2 r; r.x = __builtin_amdgcn_rcpf(d.x); r.y = __builtin_amdgcn_rcpf(d.y);
  f32x2 ge = g * r;
  return pack2v(ge * u);
}

template <int NF>
__device__ __forceinline__ void g1_epi(f32x4 (&acc)[NF][4], unsigned char* hb, int nfg0) {
  constexpr int NP = NF / 2;
  u32x2 hp[NP][4];
#pragma unroll
  for (int i = 0; i < NP; ++i) {
#pragma unroll
    for (int e = 0; e < 4; ++e) {
      hp[i][e].x = geglu2((f32x2){acc[2 * i][e][0], acc[2 * i][e][1]}, (f32x2){acc[2 * i + 1][e][0], acc[2 * i + 1][e][1]});
      hp[i][e].y = geglu2((f32x2){acc[2 * i][e][2], acc[2 * i][e][3]}, (f32x2){acc[2 * i + 1][e][2], acc[2 * i + 1][e][3]});
    }
    if (NP == 4) {
      if (i & 1) {
        unsigned char* p = hb + ((nfg0 + i) >> 1) * 8192;
#pragma unroll
        for (int m = 0; m < 4; ++m)
          st16(p + m * 1024, (u32x4){hp[i - 1][m].x, hp[i - 1][m].y, hp[i][m].x, hp[i][m].y});
      }
    } else if (i == 2) {
      if (nfg0 & 1) {
        unsigned char* p = hb + (nfg0 >> 1) * 8192 + 8;
        unsigned char* q = hb + ((nfg0 + 1) >> 1) * 8192;
#pragma unroll
        for (int m = 0; m < 4; ++m) {
          st8(p + m * 1024, hp[0][m]);
          st16(q + m * 1024, (u32x4){hp[1][m].x, hp[1][m].y, hp[2][m].x, hp[2][m].y});
        }
      } else {
        unsigned char* p = hb + ((nfg0 + 2) >> 1) * 8192;
        unsigned char* q = hb + (nfg0 >> 1) * 8192;
#pragma unroll
        for (int m = 0; m < 4; ++m) {
          st8(p + m * 1024, hp[2][m]);
          st16(q + m * 1024, (u32x4){hp[0][m].x, hp[0][m].y, hp[1][m].x, hp[1][m].y});
        }
      }
    }
  }
}

template <int NF>
__device__ __forceinline__ void g1_run(const G1Ctx& c, unsigned char* hb, int nfg0) {
  constexpr int NWL = (NF == 8) ? 3 : 4;
  f32x4 acc[NF][4];
#pragma unroll
  for (int f = 0; f < NF; ++f)
#pragma unroll
    for (int m = 0; m < 4; ++m) acc[f][m] = (f32x4){0.f, 0.f, 0.f, 0.f};
  bf16x8 xf[2][4];
  bf16x4 wlo[4], whi[4];
#pragma unroll
  for (int i = 0; i < 4; ++i) g1_issue_x(c, i, 0, 0);
#pragma unroll
  for (int i = 0; i < NWL; ++i) g1_issue_w(c, i, 0, 0);
#pragma unroll
  for (int i = 0; i < 2; ++i) g1_issue_w(c, i, 1, G1_WB);
  WAIT_VM(2);
  BAR();
#pragma unroll
  for (int m = 0; m < 4; ++m) xf[0][m] = lds_b128(c.xa[0] + m * 2048);
  wlo[0] = lds_tr(c.wa[0]); whi[0] = lds_tr(c.wa[1]);
  wlo[1] = lds_tr(c.wa[0] + 14336); whi[1] = lds_tr(c.wa[1] + 14336);
  SCHED();
#pragma unroll
  for (int i = 0; i < 4; ++i) g1_issue_x(c, i, 1, G1_XB);
  if (c.pfs1 != 0) {
#pragma unroll
    for (int i = 2; i < G1_PD; ++i) { l2pf(c.pf + (size_t)c.pfs1 * i, c.scr); l2pf(c.pf2 + (size_t)c.pfs2 * i, c.scr); }
  }
  SCHED();
  uint32_t xo0 = 0, xo1 = G1_XB, xo2 = 2 * G1_XB, wo = 0;
#pragma nounroll
  for (int t = 0; t < G1_NT; ++t) {
    g1_tile<NF>(c, t, xo0, xo1, xo2, wo, acc, xf, wlo, whi);
    const uint32_t tmp = xo0; xo0 = xo1; xo1 = xo2; xo2 = tmp;
    wo = G1_WB - wo;
  }
  g1_epi<NF>(acc, hb, nfg0);
}

constexpr int G2_NT = 28;
constexpr int G2_ST = 32768;
constexpr int G2_RING = 3 * G2_ST;

__device__ __forceinline__ void g2_issue_h(const char* hs, uint32_t hv, int st, uint32_t hdst) {
  const char* hp = hs + (size_t)st * 16384;
  glds16_sc1<0>(hp, hv, hdst);
  glds16_sc1<1024>(hp, hv, hdst);
  glds16_sc1<2048>(hp, hv, hdst);
  glds16_sc1<3072>(hp, hv, hdst);
}
__device__ __forceinline__ void g2_issue_w(const char* ws, uint32_t wv0, uint32_t wv1, int st, uint32_t wdst) {
  const char* wp = ws + (size_t)st * (64 * 2048);
  glds16(wp, wv0, wdst);
  glds16(wp + 4 * 2048, wv1, wdst + 1024);
  glds16(wp + 8 * 2048, wv0, wdst + 2048);
  glds16(wp + 12 * 2048, wv1, wdst + 3072);
}
__device__ __forceinline__ unsigned g2_need(int st) {
  const int lo = (64 * st) / 112, hi = (64 * st + 63) / 112;
  return (1u << lo) | (1u << hi);
}
__device__ __forceinline__ void g2_poll(const unsigned* myslots, int lane, unsigned need, unsigned& ready) {
  for (int spin = 0; (ready & need) != need && spin < (1 << 20); ++spin) {
    const unsigned v = __hip_atomic_load(myslots + (lane & 31), __ATOMIC_RELAXED, __HIP_MEMORY_SCOPE_AGENT);
    ready |= (unsigned)__ballot(v == 1u);
  }
  asm volatile("" ::: "memory");
}

template <bool RD, bool UPD>
__device__ __forceinline__ void g2_kb(uint32_t (&ha)[2], uint32_t (&wa)[4], uint32_t off, uint32_t delta, f32x4 (&acc)[4][4],
                                      const bf16x8 (&hc)[4], const bf16x4 (&wlc)[4], const bf16x4 (&whc)[4],
                                      bf16x8 (&hn)[4], bf16x4 (&wln)[4], bf16x4 (&whn)[4]) {
#pragma unroll
  for (int f = 0; f < 4; ++f) {
    const bf16x8 w = cat8(wlc[f], whc[f]);
#pragma unroll
    for (int a = 0; a < 4; ++a) {
      acc[f][a] = mfma16(w, hc[a], acc[f][a]);
      SCHED();
      const int g = 4 * f + a;
      if (RD) {
        if (g < 4) hn[g] = lds_b128(ha[g & 1] + off + (g >> 1) * 2048);
        else if (g < 12) { const int q = (g - 4) >> 1; if (g & 1) whn[q] = lds_tr(wa[q] + off + 4096); else wln[q] = lds_tr(wa[q] + off); }
      }
      if (UPD) {
        if (g == 12) { wa[0] += delta; wa[1] += delta; }
        if (g == 13) { wa[2] += delta; wa[3] += delta; }
        if (g == 14) { ha[0] += delta; ha[1] += delta; }
      }
      SCHED();
    }
  }
}

__device__ __forceinline__ void g2_finish(unsigned char* ws_base, int bid, int wid, int lane, f32x4 (&kep)[4], unsigned char* op) {
  const unsigned* iflag = (const unsigned*)(ws_base + WS_FLAGS + (size_t)(bid * 4 + (wid & 3)) * 64);
  for (int spin = 0; spin < (1 << 22); ++spin) {
    const unsigned v = __builtin_amdgcn_readfirstlane(__hip_atomic_load(iflag, __ATOMIC_RELAXED, __HIP_MEMORY_SCOPE_AGENT));
    if (v == 1u) break;
    __builtin_amdgcn_s_sleep(1);
  }
  asm volatile("" ::: "memory");
  const unsigned char* xr = ws_base + WS_XCH + (size_t)(bid * 8 + wid) * 4096 + lane * 16;
  f32x4 o0, o1, o2, o3;
  asm volatile("global_load_dwordx4 %0, %4, off sc1\n\t"
               "global_load_dwordx4 %1, %4, off offset:1024 sc1\n\t"
               "global_load_dwordx4 %2, %4, off offset:2048 sc1\n\t"
               "global_load_dwordx4 %3, %4, off offset:3072 sc1\n\t"
               "s_waitcnt vmcnt(0)"
               : "=&v"(o0), "=&v"(o1), "=&v"(o2), "=&v"(o3) : "v"(xr) : "memory");
  kep[0] += o0; kep[1] += o1; kep[2] += o2; kep[3] += o3;
#pragma unroll
  for (int f = 0; f < 4; ++f) {
    const f32x4 v = kep[f];
    u32x2 pk; pk.x = pack2v((f32x2){v[0], v[1]}); pk.y = pack2v((f32x2){v[2], v[3]});
    *(u32x2*)(op + f * 32) = pk;
  }
}

__global__ __launch_bounds__(512, 2) void ffn_kernel(const unsigned char* __restrict__ x, const unsigned char* __restrict__ wg,
                                                     const unsigned char* __restrict__ wu, const unsigned char* __restrict__ wd,
                                                     unsigned char* __restrict__ out, unsigned char* __restrict__ Hf) {
  __shared__ __attribute__((aligned(1024))) unsigned char smem[G1_LDS];
  const uint32_t lbase = (uint32_t)(uintptr_t)(LAS unsigned char*)smem;
  if (__builtin_amdgcn_readfirstlane(threadIdx.x) >= 320)
    l2pf((const char*)__builtin_amdgcn_s_getpc() + (size_t)(threadIdx.x - 320) * CODE_PF_STRIDE, lbase + G1_SCR);
  SCHED();
  const int bid = blockIdx.x;
  const int xcd = bid & 7, idx = bid >> 3;
  const int pm = 4 * (xcd & 1) + (idx & 3), pn = 8 * (xcd >> 1) + (idx >> 2);
  const int brow = pm * 256;
  const int lane = threadIdx.x & 63;
  const int wid = __builtin_amdgcn_readfirstlane(threadIdx.x >> 6);
  const int wr = wid & 3, wc = wid >> 2;

  const char* pfa; const char* pfb; uint32_t pfbs;
  {
    const int li = idx * 56 + (lane < 56 ? lane : 0);
    const int mat = li / 896, r = (li % 896) / 14, cc = li % 14;
    pfa = (const char*)(mat ? wu : wg) + (size_t)r * (F_ * 2) + (xcd >> 1) * 1792 + cc * 128;
    const char* px = (const char*)x + (size_t)(brow + 32 * (pn & 7) + (lane & 31)) * 2048;
    if (wid < 4) {
      if (wid & 1) l2pf(pfa + (size_t)(64 * F_ * 2) * (wid >> 1), lbase + G1_SCR);
      else l2pf(px + 128 * (wid >> 1), lbase + G1_SCR);
    }
    if (lane >= 32 && lane < 48) {
      pfb = (const char*)wd + (size_t)((bid * 16 + (lane - 32)) * 128) - (size_t)2 * (256 * 2048);
      pfbs = 256 * 2048;
    } else {
      pfb = px;
      pfbs = 128;
    }
  }
  unsigned* myslots = (unsigned*)(Hf + WS_SLOTS) + (size_t)(pm * 32 + pn) * 32;
  if (wid == 4) {
    if (lane < 32) __hip_atomic_store(myslots + lane, 0u, __ATOMIC_RELAXED, __HIP_MEMORY_SCOPE_AGENT);
    else if (lane < 36) __hip_atomic_store((unsigned*)(Hf + WS_FLAGS + (size_t)(bid * 4 + lane - 32) * 64), 0u, __ATOMIC_RELAXED, __HIP_MEMORY_SCOPE_AGENT);
  }

  {
    G1Ctx c;
    c.xs = (const char*)x + (size_t)(brow + 32 * wid) * 2048;
#pragma unroll
    for (int a = 0; a < 2; ++a) {
      const int r = 8 * a + (lane >> 3);
      const int ch = (lane & 7) ^ ((4 * a + (lane >> 4)) & 7);
      c.xv[a] = r * 2048 + ch * 16;
    }
    c.xl = lbase + wid * 4096;
    const int e0 = (wid < 4) ? 3 * wid : 12 + 4 * (wid - 4);
#pragma unroll
    for (int j = 0; j < 4; ++j) {
      int e = e0 + j; if (e > 27) e = 27;
      const int mat = e / 14, jj = e % 14;
      const int ci = 64 * jj + lane;
      const int rowp = ci / 14, cc = ci % 14;
      const int row = rowp ^ (((rowp >> 3) & 1) << 2);
      c.wv[j] = row * (F_ * 2) + cc * 16;
      c.ws[j] = (const char*)(mat ? wu : wg) + pn * 224;
      c.wl[j] = lbase + G1_WOFF + mat * 14336 + jj * 1024;
    }
    c.pf = pfa; c.pf2 = pfb; c.pfs1 = 64 * F_ * 2; c.pfs2 = pfbs;
    if (wid != 4) { c.pf = (const char*)x; c.pf2 = (const char*)x; c.pfs1 = 0; c.pfs2 = 0; }
    c.scr = lbase + G1_SCR;
    const int g = lane >> 4, r16 = lane & 15;
#pragma unroll
    for (int s = 0; s < 2; ++s)
      c.xa[s] = lbase + (wr * 64 + r16) * 128 + (((4 * s + g) ^ (r16 >> 1)) & 7) * 16;
    const int q = r16 >> 2, p = lane & 3;
    const int fbase = (wc == 0) ? ((pn & 1) ? 3 : 0) : ((pn & 1) ? 0 : 4);
#pragma unroll
    for (int i = 0; i < 2; ++i)
      c.wa[i] = lbase + G1_WOFF + (8 * g + 4 * (i ^ (g & 1)) + q) * 224 + fbase * 32 + p * 8;
    const int nfg0 = 7 * pn + fbase;
    unsigned char* hb = Hf + (size_t)(2 * pm + (wr >> 1)) * (112 * 8192) + (wr & 1) * 4096 + lane * 16;
    if (wc == 0) g1_run<8>(c, hb, nfg0);
    else g1_run<6>(c, hb, nfg0);
  }
  WAIT_LGKM0();
  WAIT_VM(0);
  BAR();

  const int kh2 = pn >> 4, cp = ((pn >> 3) & 1) * 4 + ((pn >> 1) & 3), rp = 2 * pm + (pn & 1);
  const int s0 = ((112 * (pn & 15) + 63) / 64) % G2_NT;
  const int pbid = bid ^ 4;
  const int wm = wr & 1, wn = wr >> 1;
  unsigned char* op = out + (size_t)(rp * 128 + wm * 64 + wc * 32 + kh2 * 16 + (lane & 15)) * 2048 + cp * 256 + wn * 128 + (lane >> 4) * 8;
  const uint32_t xl = lbase + G2_RING + wr * 4096 + lane * 16;

  if (wc == 1) {
    const int l = wr;
    const char* hs = (const char*)Hf + (size_t)(rp * 112 + kh2 * 56) * 8192 + l * 4096;
    const char* ws = (const char*)wd + (size_t)(kh2 * 1792 + 16 * l) * 2048 + cp * 256;
    const uint32_t hv = lane * 16;
    const int pc = lane & 15, r4 = lane >> 4;
    const uint32_t wv0 = r4 * 2048 + ((((pc >> 1) ^ r4) & 7) * 2 + (pc & 1)) * 16;
    const uint32_t wv1 = r4 * 2048 + ((((pc >> 1) ^ (4 + r4)) & 7) * 2 + (pc & 1)) * 16;
    const uint32_t hb = lbase + l * 4096, wb = lbase + 16384 + l * 4096;
    unsigned* dst = (unsigned*)(Hf + WS_SLOTS) + (size_t)(pm * 32 + (lane & 31)) * 32 + pn;
    unsigned ready = 1u << pn;
    if (l == 0 && lane < 32) __hip_atomic_store(dst, 1u, __ATOMIC_RELAXED, __HIP_MEMORY_SCOPE_AGENT);
    int sb = s0 + 1; if (sb >= G2_NT) sb -= G2_NT;
    int sc = sb + 1; if (sc >= G2_NT) sc -= G2_NT;
    g2_issue_h(hs, hv, s0, hb);
    g2_issue_w(ws, wv0, wv1, s0, wb);
    g2_issue_w(ws, wv0, wv1, sb, wb + G2_ST);
    g2_issue_w(ws, wv0, wv1, sc, wb + 2 * G2_ST);
    WAIT_VM(8);
    BAR();
    g2_poll(myslots, lane, (g2_need(sb) | g2_need(sc)) << (16 * kh2), ready);
    g2_issue_h(hs, hv, sb, hb + G2_ST);
    g2_issue_h(hs, hv, sc, hb + 2 * G2_ST);
    int st = sc;
    uint32_t so = 0;
    for (int t = 0; t < G2_NT - 3; ++t) {
      ++st; if (st >= G2_NT) st -= G2_NT;
      if (l == 0) g2_poll(myslots, lane, g2_need(st) << (16 * kh2), ready);
      if (t == 0) WAIT_VM(4); else WAIT_VM(8);
      BAR();
      g2_issue_h(hs, hv, st, hb + so);
      g2_issue_w(ws, wv0, wv1, st, wb + so);
      so += G2_ST; if (so == G2_RING) so = 0;
    }
    WAIT_VM(8);
    BAR();
    WAIT_VM(0);
    BAR();
    if (l == 0) {
      g2_poll(myslots, lane, 0xffffffffu, ready);
      if (lane < 32) __hip_atomic_store(dst, 1u, __ATOMIC_RELAXED, __HIP_MEMORY_SCOPE_AGENT);
    }
    BAR();
    f32x4 kep[4];
#pragma unroll
    for (int f = 0; f < 4; ++f) kep[f] = *(LAS f32x4*)(xl + f * 1024);
    g2_finish(Hf, bid, wid, lane, kep, op);
    return;
  }

  uint32_t wa0[4];
  {
    const int g = lane >> 4, q = (lane >> 2) & 3, p = lane & 3;
    const int r = 4 * g + q;
#pragma unroll
    for (int f = 0; f < 4; ++f)
      wa0[f] = lbase + 16384 + r * 256 + (((4 * wn + f) ^ r) & 7) * 32 + p * 8;
  }
  uint32_t ha0[2];
#pragma unroll
  for (int a = 0; a < 2; ++a) ha0[a] = lbase + wm * 4096 + (a ^ kh2) * 1024 + lane * 16;

  f32x4 acc[4][4];
#pragma unroll
  for (int f = 0; f < 4; ++f)
#pragma unroll
    for (int a = 0; a < 4; ++a) acc[f][a] = (f32x4){0.f, 0.f, 0.f, 0.f};
  bf16x8 hf0[4], hf1[4];
  bf16x4 wl0[4], wh0[4], wl1[4], wh1[4];

  BAR();
#pragma unroll
  for (int i = 0; i < 4; ++i) hf0[i] = lds_b128(ha0[i & 1] + (i >> 1) * 2048);
#pragma unroll
  for (int i = 0; i < 4; ++i) { wl0[i] = lds_tr(wa0[i]); wh0[i] = lds_tr(wa0[i] + 4096); }
  int slot = 0;
  for (int t = 0; t < G2_NT - 1; ++t) {
    const uint32_t delta = (slot == 2) ? (uint32_t)(-2 * G2_ST) : (uint32_t)G2_ST;
    slot = (slot == 2) ? 0 : slot + 1;
    g2_kb<true, true>(ha0, wa0, 8192, delta, acc, hf0, wl0, wh0, hf1, wl1, wh1);
    WAIT_LGKM0();
    BAR();
    g2_kb<true, false>(ha0, wa0, 0, 0, acc, hf1, wl1, wh1, hf0, wl0, wh0);
  }
  g2_kb<true, false>(ha0, wa0, 8192, 0, acc, hf0, wl0, wh0, hf1, wl1, wh1);
  g2_kb<false, false>(ha0, wa0, 0, 0, acc, hf1, wl1, wh1, hf0, wl0, wh0);
  SCHED();
  asm volatile("s_nop 15" ::: "memory");
  SCHED();
  {
    unsigned char* xs = Hf + WS_XCH + (size_t)(pbid * 8 + wid) * 4096 + lane * 16;
#pragma unroll
    for (int f = 0; f < 4; ++f) {
      st16(xs + f * 1024, __builtin_bit_cast(u32x4, acc[f][1]));
      st16(xs + 16384 + f * 1024, __builtin_bit_cast(u32x4, acc[f][3]));
    }
  }
#pragma unroll
  for (int f = 0; f < 4; ++f) *(LAS f32x4*)(xl + f * 1024) = acc[f][2];
  WAIT_LGKM0();
  BAR();
  WAIT_VM(0);
  if (lane == 0) __hip_atomic_store((unsigned*)(Hf + WS_FLAGS + (size_t)(pbid * 4 + wid) * 64), 1u, __ATOMIC_RELAXED, __HIP_MEMORY_SCOPE_AGENT);
  {
    f32x4 kep[4];
#pragma unroll
    for (int f = 0; f < 4; ++f) kep[f] = acc[f][0];
    g2_finish(Hf, bid, wid, lane, kep, op);
  }
}

extern "C" void kernel_launch(void* const* d_in, const int* in_sizes, int n_in,
                              void* d_out, int out_size, void* d_ws, size_t ws_size,
                              hipStream_t stream) {
  const unsigned char* x = (const unsigned char*)d_in[0];
  const unsigned char* wg = (const unsigned char*)d_in[1];
  const unsigned char* wu = (const unsigned char*)d_in[2];
  const unsigned char* wd = (const unsigned char*)d_in[3];
  ffn_kernel<<<256, 512, 0, stream>>>(x, wg, wu, wd, (unsigned char*)d_out, (unsigned char*)d_ws);
}
```

```cpp
#include <hip/hip_runtime.h>
#include <hip/hip_bf16.h>
#include <stdint.h>

typedef __bf16 bf16x8 __attribute__((ext_vector_type(8)));
typedef __bf16 bf16x4 __attribute__((ext_vector_type(4)));
typedef __bf16 bf16x2 __attribute__((ext_vector_type(2)));
typedef float f32x4 __attribute__((ext_vector_type(4)));
typedef float f32x2 __attribute__((ext_vector_type(2)));
typedef unsigned u32x2 __attribute__((ext_vector_type(2)));
typedef unsigned u32x4 __attribute__((ext_vector_type(4)));
#define LAS __attribute__((address_space(3)))

#define D_ 1024
#define F_ 3584

__device__ __forceinline__ void glds16(const void* sbase, uint32_t voff, uint32_t lds_addr) {
  asm volatile("s_mov_b32 m0, %2\n\ts_nop 0\n\tglobal_load_lds_dwordx4 %0, %1"
               :: "v"(voff), "s"(sbase), "s"(lds_addr) : "memory");
}
template <int OFF>
__device__ __forceinline__ void glds16_sc1(const void* sbase, uint32_t voff, uint32_t lds_addr) {
  asm volatile("s_mov_b32 m0, %2\n\ts_nop 0\n\tglobal_load_lds_dwordx4 %0, %1 offset:%3 sc1"
               :: "v"(voff), "s"(sbase), "s"(lds_addr), "n"(OFF) : "memory");
}
__device__ __forceinline__ void l2pf(const char* addr, uint32_t lds_scratch) {
  asm volatile("s_mov_b32 m0, %1\n\ts_nop 0\n\tglobal_load_lds_dword %0, off"
               :: "v"(addr), "s"(lds_scratch) : "memory");
}
__device__ __forceinline__ void st16(unsigned char* p, u32x4 v) {
  asm volatile("global_store_dwordx4 %0, %1, off sc1\n\ts_nop 1" :: "v"(p), "v"(v) : "memory");
}
#define WAIT_VM(n) asm volatile("s_waitcnt vmcnt(" #n ")" ::: "memory")
#define WAIT_LGKM0() asm volatile("s_waitcnt lgkmcnt(0)" ::: "memory")
#define BAR() do { asm volatile("" ::: "memory"); __builtin_amdgcn_s_barrier(); asm volatile("" ::: "memory"); } while (0)
#define SCHED() __builtin_amdgcn_sched_barrier(0)

__device__ __forceinline__ bf16x8 lds_b128(uint32_t addr) { return *(LAS bf16x8*)(addr); }
__device__ __forceinline__ bf16x4 lds_tr(uint32_t a) { return __builtin_amdgcn_ds_read_tr16_b64_v4bf16((LAS bf16x4*)(a)); }
__device__ __forceinline__ bf16x8 cat8(bf16x4 lo, bf16x4 hi) { return __builtin_shufflevector(lo, hi, 0, 1, 2, 3, 4, 5, 6, 7); }
__device__ __forceinline__ f32x4 mfma16(bf16x8 a, bf16x8 b, f32x4 c) {
  return __builtin_amdgcn_mfma_f32_16x16x32_bf16(a, b, c, 0, 0, 0);
}
__device__ __forceinline__ unsigned pack2v(f32x2 v) {
  return __builtin_bit_cast(unsigned, __builtin_convertvector(v, bf16x2));
}

constexpr size_t WS_FLAGS = (size_t)16 << 20;
constexpr size_t WS_SLOTS = (size_t)17 << 20;
constexpr size_t WS_XCH = (size_t)32 << 20;

constexpr int G1_XB = 32768;
constexpr int G1_WB = 28672;
constexpr int G1_WOFF = 3 * G1_XB;
constexpr int G1_SCR = 3 * G1_XB + 2 * G1_WB;
constexpr int G1_LDS = G1_SCR + 256;
constexpr int G1_PD = 4;
constexpr int G1_NT = D_ / 64;
constexpr int CODE_PF_STRIDE = 104;

struct G1Ctx {
  const char* xs;
  const char* ws[4];
  uint32_t xv[2];
  uint32_t wv[4];
  uint32_t xl;
  uint32_t wl[4];
  uint32_t xa[2];
  uint32_t wa[2];
  const char* pf;
  const char* pf2;
  uint32_t pfs1, pfs2;
  uint32_t scr;
};

__device__ __forceinline__ void g1_issue_x(const G1Ctx& c, int idx, int t, uint32_t xoff) {
  glds16(c.xs + (idx >> 1) * (16 * 2048) + t * 128, c.xv[idx & 1], c.xl + xoff + idx * 1024);
}
__device__ __forceinline__ void g1_issue_w(const G1Ctx& c, int j, int t, uint32_t woff) {
  glds16(c.ws[j] + (size_t)t * (64 * F_ * 2), c.wv[j], c.wl[j] + woff);
}

template <int NF>
__device__ __forceinline__ void g1_tile(const G1Ctx& c, int t, uint32_t xo_cur, uint32_t xo_nxt, uint32_t xo_free, uint32_t wo_cur,
                                        f32x4 (&acc)[NF][4], bf16x8 (&xf)[2][4], bf16x4 (&wlo)[4], bf16x4 (&whi)[4]) {
  constexpr int NWL = (NF == 8) ? 3 : 4;
  constexpr int NS = 2 * NF;
  const int t1 = (t + 1 < G1_NT) ? t + 1 : G1_NT - 1;
  const int t2 = (t + 2 < G1_NT) ? t + 2 : G1_NT - 1;
  const int tp = (t + G1_PD < G1_NT) ? t + G1_PD : G1_NT - 1;
  const uint32_t wo_nxt = G1_WB - wo_cur;
  const uint32_t xa1 = c.xa[1] + xo_cur;
  const uint32_t xa0n = c.xa[0] + xo_nxt;
  const uint32_t wac0 = c.wa[0] + wo_cur, wac1 = c.wa[1] + wo_cur;
  const uint32_t wan0 = c.wa[0] + wo_nxt, wan1 = c.wa[1] + wo_nxt;
#pragma unroll
  for (int j = 0; j < NS; ++j) {
    const int s = j / NF, f = j % NF;
    if (j == NS - 2) {
      WAIT_LGKM0();
      WAIT_VM(6);
      BAR();
    }
    uint32_t a0, a1;
    if (j + 2 < NS) {
      const int s2 = (j + 2) / NF, f2 = (j + 2) % NF;
      const uint32_t o = s2 * (32 * 224) + (f2 & 1) * 14336 + (f2 >> 1) * 32;
      a0 = wac0 + o; a1 = wac1 + o;
    } else {
      const int f2 = j + 2 - NS;
      const uint32_t o = (f2 & 1) * 14336 + (f2 >> 1) * 32;
      a0 = wan0 + o; a1 = wan1 + o;
    }
    const bf16x8 w = cat8(wlo[j & 3], whi[j & 3]);
    acc[f][0] = mfma16(w, xf[s][0], acc[f][0]);
    SCHED();
    wlo[(j + 2) & 3] = lds_tr(a0);
    SCHED();
    acc[f][1] = mfma16(w, xf[s][1], acc[f][1]);
    SCHED();
    whi[(j + 2) & 3] = lds_tr(a1);
    SCHED();
    acc[f][2] = mfma16(w, xf[s][2], acc[f][2]);
    SCHED();
    if (j < 4) xf[1][j] = lds_b128(xa1 + j * 2048);
    if (j >= NS - 2) {
      const int m0 = 2 * (j - (NS - 2));
      xf[0][m0] = lds_b128(xa0n + m0 * 2048);
      xf[0][m0 + 1] = lds_b128(xa0n + (m0 + 1) * 2048);
    }
    SCHED();
    acc[f][3] = mfma16(w, xf[s][3], acc[f][3]);
    SCHED();
    if (j >= NS - 2) g1_issue_w(c, j - (NS - 2), t2, wo_cur);
    if (j + 2 < NWL) g1_issue_w(c, j + 2, t1, wo_nxt);
    if (j == 2) { l2pf(c.pf + (size_t)c.pfs1 * tp, c.scr); l2pf(c.pf2 + (size_t)c.pfs2 * tp, c.scr); }
    if (j >= 3 && j < 7) g1_issue_x(c, j - 3, t2, xo_free);
    SCHED();
  }
}

__device__ __forceinline__ unsigned geglu2(f32x2 g, f32x2 u) {
  const float c1 = -2.0f * 0.7978845608028654f * 1.4426950408889634f;
  const float c2 = c1 * 0.044715f;
  f32x2 p = (g * g) * c2 + c1;
  f32x2 a = g * p;
  f32x2 e; e.x = __builtin_amdgcn_exp2f(a.x); e.y = __builtin_amdgcn_exp2f(a.y);
  f32x2 d = e + 1.0f;
  f32x2 r; r.x = __builtin_amdgcn_rcpf(d.x); r.y = __builtin_amdgcn_rcpf(d.y);
  f32x2 ge = g * r;
  return pack2v(ge * u);
}

template <int NF>
__device__ __forceinline__ void g1_epi(f32x4 (&acc)[NF][4], unsigned char* hb, int nfg0, int lane) {
  constexpr int NP = NF / 2;
  u32x2 hp[NP][4];
#pragma unroll
  for (int i = 0; i < NP; ++i) {
#pragma unroll
    for (int e = 0; e < 4; ++e) {
      hp[i][e].x = geglu2((f32x2){acc[2 * i][e][0], acc[2 * i][e][1]}, (f32x2){acc[2 * i + 1][e][0], acc[2 * i + 1][e][1]});
      hp[i][e].y = geglu2((f32x2){acc[2 * i][e][2], acc[2 * i][e][3]}, (f32x2){acc[2 * i + 1][e][2], acc[2 * i + 1][e][3]});
    }
    if (NP == 4) {
      if (i & 1) {
        unsigned char* p = hb + ((nfg0 + i) >> 1) * 8192;
#pragma unroll
        for (int m = 0; m < 4; ++m)
          st16(p + m * 1024, (u32x4){hp[i - 1][m].x, hp[i - 1][m].y, hp[i][m].x, hp[i][m].y});
      }
    } else if (i == 2) {
      const int half = nfg0 & 1;
      unsigned char* q = hb + ((nfg0 + half) >> 1) * 8192;
      unsigned char* p = hb - lane * 16 + ((nfg0 + (half ? 0 : 2)) >> 1) * 8192 + (((lane >> 5) + 2 * half) * 16 + (lane & 15)) * 16;
#pragma unroll
      for (int m = 0; m < 4; ++m) {
        const u32x2 sv = half ? hp[0][m] : hp[2][m];
        const u32x2 f0 = half ? hp[1][m] : hp[0][m], f1 = half ? hp[2][m] : hp[1][m];
        st16(q + m * 1024, (u32x4){f0.x, f0.y, f1.x, f1.y});
        const unsigned nx = __shfl_down(sv.x, 16), ny = __shfl_down(sv.y, 16);
        if ((lane & 16) == 0) st16(p + m * 1024, (u32x4){sv.x, sv.y, nx, ny});
      }
    }
  }
}

template <int NF>
__device__ __forceinline__ void g1_run(const G1Ctx& c, unsigned char* hb, int nfg0, int lane) {
  constexpr int NWL = (NF == 8) ? 3 : 4;
  f32x4 acc[NF][4];
#pragma unroll
  for (int f = 0; f < NF; ++f)
#pragma unroll
    for (int m = 0; m < 4; ++m) acc[f][m] = (f32x4){0.f, 0.f, 0.f, 0.f};
  bf16x8 xf[2][4];
  bf16x4 wlo[4], whi[4];
#pragma unroll
  for (int i = 0; i < 4; ++i) g1_issue_x(c, i, 0, 0);
#pragma unroll
  for (int i = 0; i < NWL; ++i) g1_issue_w(c, i, 0, 0);
#pragma unroll
  for (int i = 0; i < 2; ++i) g1_issue_w(c, i, 1, G1_WB);
  WAIT_VM(2);
  BAR();
#pragma unroll
  for (int m = 0; m < 4; ++m) xf[0][m] = lds_b128(c.xa[0] + m * 2048);
  wlo[0] = lds_tr(c.wa[0]); whi[0] = lds_tr(c.wa[1]);
  wlo[1] = lds_tr(c.wa[0] + 14336); whi[1] = lds_tr(c.wa[1] + 14336);
  SCHED();
#pragma unroll
  for (int i = 0; i < 4; ++i) g1_issue_x(c, i, 1, G1_XB);
  if (c.pfs1 != 0) {
#pragma unroll
    for (int i = 2; i < G1_PD; ++i) { l2pf(c.pf + (size_t)c.pfs1 * i, c.scr); l2pf(c.pf2 + (size_t)c.pfs2 * i, c.scr); }
  }
  SCHED();
  uint32_t xo0 = 0, xo1 = G1_XB, xo2 = 2 * G1_XB, wo = 0;
#pragma nounroll
  for (int t = 0; t < G1_NT; ++t) {
    g1_tile<NF>(c, t, xo0, xo1, xo2, wo, acc, xf, wlo, whi);
    const uint32_t tmp = xo0; xo0 = xo1; xo1 = xo2; xo2 = tmp;
    wo = G1_WB - wo;
  }
  g1_epi<NF>(acc, hb, nfg0, lane);
}

constexpr int G2_NT = 28;
constexpr int G2_ST = 32768;
constexpr int G2_RING = 3 * G2_ST;

__device__ __forceinline__ void g2_issue_h(const char* hs, uint32_t hv, int st, uint32_t hdst) {
  const char* hp = hs + (size_t)st * 16384;
  glds16_sc1<0>(hp, hv, hdst);
  glds16_sc1<1024>(hp, hv, hdst);
  glds16_sc1<2048>(hp, hv, hdst);
  glds16_sc1<3072>(hp, hv, hdst);
}
__device__ __forceinline__ void g2_issue_w(const char* ws, uint32_t wv0, uint32_t wv1, int st, int kb0, int i, uint32_t wdst) {
  const bool shared = ((kb0 + 2 * st) % 7) == 3;
  const char* wp = ws + (size_t)st * (64 * 2048) + (shared ? 4 * i : 16 * i) * 2048;
  const int rs = (shared ? 8 : 4) * 2048;
  glds16(wp, wv0, wdst);
  glds16(wp + rs, wv1, wdst + 1024);
  glds16(wp + 2 * rs, wv0, wdst + 2048);
  glds16(wp + 3 * rs, wv1, wdst + 3072);
}
__device__ __forceinline__ unsigned g2_need(int st) {
  const int lo = (64 * st) / 112, hi = (64 * st + 63) / 112;
  return (1u << lo) | (1u << hi);
}
__device__ __forceinline__ void g2_poll(const unsigned* myslots, int lane, unsigned need, unsigned& ready) {
  for (int spin = 0; (ready & need) != need && spin < (1 << 20); ++spin) {
    const unsigned v = __hip_atomic_load(myslots + (lane & 31), __ATOMIC_RELAXED, __HIP_MEMORY_SCOPE_AGENT);
    ready |= (unsigned)__ballot(v == 1u);
  }
  asm volatile("" ::: "memory");
}

template <bool RD, bool UPD>
__device__ __forceinline__ void g2_kb(uint32_t (&ha)[2], uint32_t (&wa)[4], uint32_t off, uint32_t delta, f32x4 (&acc)[4][4],
                                      const bf16x8 (&hc)[4], const bf16x4 (&wlc)[4], const bf16x4 (&whc)[4],
                                      bf16x8 (&hn)[4], bf16x4 (&wln)[4], bf16x4 (&whn)[4]) {
#pragma unroll
  for (int f = 0; f < 4; ++f) {
    const bf16x8 w = cat8(wlc[f], whc[f]);
#pragma unroll
    for (int a = 0; a < 4; ++a) {
      acc[f][a] = mfma16(w, hc[a], acc[f][a]);
      SCHED();
      const int g = 4 * f + a;
      if (RD) {
        if (g < 4) hn[g] = lds_b128(ha[g & 1] + off + (g >> 1) * 2048);
        else if (g < 12) { const int q = (g - 4) >> 1; if (g & 1) whn[q] = lds_tr(wa[q] + off + 4096); else wln[q] = lds_tr(wa[q] + off); }
      }
      if (UPD) {
        if (g == 12) { wa[0] += delta; wa[1] += delta; }
        if (g == 13) { wa[2] += delta; wa[3] += delta; }
        if (g == 14) { ha[0] += delta; ha[1] += delta; }
      }
      SCHED();
    }
  }
}

__device__ __forceinline__ void g2_finish(unsigned char* ws_base, int bid, int wid, int lane, f32x4 (&kep)[4], unsigned char* op) {
  const unsigned* iflag = (const unsigned*)(ws_base + WS_FLAGS + (size_t)(bid * 4 + (wid & 3)) * 64);
  for (int spin = 0; spin < (1 << 22); ++spin) {
    const unsigned v = __builtin_amdgcn_readfirstlane(__hip_atomic_load(iflag, __ATOMIC_RELAXED, __HIP_MEMORY_SCOPE_AGENT));
    if (v == 1u) break;
    __builtin_amdgcn_s_sleep(1);
  }
  asm volatile("" ::: "memory");
  const unsigned char* xr = ws_base + WS_XCH + (size_t)(bid * 8 + wid) * 4096 + lane * 16;
  f32x4 o0, o1, o2, o3;
  asm volatile("global_load_dwordx4 %0, %4, off sc1\n\t"
               "global_load_dwordx4 %1, %4, off offset:1024 sc1\n\t"
               "global_load_dwordx4 %2, %4, off offset:2048 sc1\n\t"
               "global_load_dwordx4 %3, %4, off offset:3072 sc1\n\t"
               "s_waitcnt vmcnt(0)"
               : "=&v"(o0), "=&v"(o1), "=&v"(o2), "=&v"(o3) : "v"(xr) : "memory");
  kep[0] += o0; kep[1] += o1; kep[2] += o2; kep[3] += o3;
#pragma unroll
  for (int f = 0; f < 4; ++f) {
    const f32x4 v = kep[f];
    u32x2 pk; pk.x = pack2v((f32x2){v[0], v[1]}); pk.y = pack2v((f32x2){v[2], v[3]});
    *(u32x2*)(op + f * 32) = pk;
  }
}

__global__ __launch_bounds__(512, 2) void ffn_kernel(const unsigned char* __restrict__ x, const unsigned char* __restrict__ wg,
                                                     const unsigned char* __restrict__ wu, const unsigned char* __restrict__ wd,
                                                     unsigned char* __restrict__ out, unsigned char* __restrict__ Hf) {
  __shared__ __attribute__((aligned(1024))) unsigned char smem[G1_LDS];
  const uint32_t lbase = (uint32_t)(uintptr_t)(LAS unsigned char*)smem;
  if (__builtin_amdgcn_readfirstlane(threadIdx.x) >= 320)
    l2pf((const char*)__builtin_amdgcn_s_getpc() + (size_t)(threadIdx.x - 320) * CODE_PF_STRIDE, lbase + G1_SCR);
  SCHED();
  const int bid = blockIdx.x;
  const int xcd = bid & 7, idx = bid >> 3;
  const int pm = 4 * (xcd & 1) + (idx & 3), pn = 8 * (xcd >> 1) + (idx >> 2);
  const int brow = pm * 256;
  const int lane = threadIdx.x & 63;
  const int wid = __builtin_amdgcn_readfirstlane(threadIdx.x >> 6);
  const int wr = wid & 3, wc = wid >> 2;

  const char* pfa; const char* pfb; uint32_t pfbs;
  {
    const int li = idx * 56 + (lane < 56 ? lane : 0);
    const int mat = li / 896, r = (li % 896) / 14, cc = li % 14;
    pfa = (const char*)(mat ? wu : wg) + (size_t)r * (F_ * 2) + (xcd >> 1) * 1792 + cc * 128;
    const char* px = (const char*)x + (size_t)(brow + 32 * (pn & 7) + (lane & 31)) * 2048;
    if (wid < 4) {
      if (wid & 1) l2pf(pfa + (size_t)(64 * F_ * 2) * (wid >> 1), lbase + G1_SCR);
      else l2pf(px + 128 * (wid >> 1), lbase + G1_SCR);
    }
    if (lane >= 32 && lane < 48) {
      pfb = (const char*)wd + (size_t)((bid * 16 + (lane - 32)) * 128) - (size_t)2 * (256 * 2048);
      pfbs = 256 * 2048;
    } else {
      pfb = px;
      pfbs = 128;
    }
  }
  unsigned* myslots = (unsigned*)(Hf + WS_SLOTS) + (size_t)(pm * 32 + pn) * 32;
  if (wid == 4) {
    if (lane < 32) __hip_atomic_store(myslots + lane, 0u, __ATOMIC_RELAXED, __HIP_MEMORY_SCOPE_AGENT);
    else if (lane < 36) __hip_atomic_store((unsigned*)(Hf + WS_FLAGS + (size_t)(bid * 4 + lane - 32) * 64), 0u, __ATOMIC_RELAXED, __HIP_MEMORY_SCOPE_AGENT);
  }

  {
    G1Ctx c;
    c.xs = (const char*)x + (size_t)(brow + 32 * wid) * 2048;
#pragma unroll
    for (int a = 0; a < 2; ++a) {
      const int r = 8 * a + (lane >> 3);
      const int ch = (lane & 7) ^ ((4 * a + (lane >> 4)) & 7);
      c.xv[a] = r * 2048 + ch * 16;
    }
    c.xl = lbase + wid * 4096;
    const int e0 = (wid < 4) ? 3 * wid : 12 + 4 * (wid - 4);
#pragma unroll
    for (int j = 0; j < 4; ++j) {
      int e = e0 + j; if (e > 27) e = 27;
      const int mat = e / 14, jj = e % 14;
      const int ci = 64 * jj + lane;
      const int rowp = ci / 14, cc = ci % 14;
      const int row = rowp ^ (((rowp >> 3) & 1) << 2);
      c.wv[j] = row * (F_ * 2) + cc * 16;
      c.ws[j] = (const char*)(mat ? wu : wg) + pn * 224;
      c.wl[j] = lbase + G1_WOFF + mat * 14336 + jj * 1024;
    }
    c.pf = pfa; c.pf2 = pfb; c.pfs1 = 64 * F_ * 2; c.pfs2 = pfbs;
    if (wid != 4) { c.pf = (const char*)x; c.pf2 = (const char*)x; c.pfs1 = 0; c.pfs2 = 0; }
    c.scr = lbase + G1_SCR;
    const int g = lane >> 4, r16 = lane & 15;
#pragma unroll
    for (int s = 0; s < 2; ++s)
      c.xa[s] = lbase + (wr * 64 + r16) * 128 + (((4 * s + g) ^ (r16 >> 1)) & 7) * 16;
    const int q = r16 >> 2, p = lane & 3;
    const int fbase = (wc == 0) ? ((pn & 1) ? 3 : 0) : ((pn & 1) ? 0 : 4);
#pragma unroll
    for (int i = 0; i < 2; ++i)
      c.wa[i] = lbase + G1_WOFF + (8 * g + 4 * (i ^ (g & 1)) + q) * 224 + fbase * 32 + p * 8;
    const int nfg0 = 7 * pn + fbase;
    unsigned char* hb = Hf + (size_t)(2 * pm + (wr >> 1)) * (112 * 8192) + (wr & 1) * 4096 + lane * 16;
    if (wc == 0) g1_run<8>(c, hb, nfg0, lane);
    else g1_run<6>(c, hb, nfg0, lane);
  }
  WAIT_LGKM0();
  WAIT_VM(0);
  BAR();

  const int kh2 = pn >> 4, cp = ((pn >> 3) & 1) * 4 + ((pn >> 1) & 3), rp = 2 * pm + (pn & 1);
  const int s0 = ((112 * (pn & 15) + 63) / 64) % G2_NT;
  const int pbid = bid ^ 4;
  const int wm = wr & 1, wn = wr >> 1;
  unsigned char* op = out + (size_t)(rp * 128 + wm * 64 + wc * 32 + kh2 * 16 + (lane & 15)) * 2048 + cp * 256 + wn * 128 + (lane >> 4) * 8;
  const uint32_t xl = lbase + G2_RING + wr * 4096 + lane * 16;

  if (wc == 1) {
    const int l = wr;
    const char* hs = (const char*)Hf + (size_t)(rp * 112 + kh2 * 56) * 8192 + l * 4096;
    const char* ws = (const char*)wd + (size_t)(kh2 * 1792 + 32 * (l >> 1)) * 2048 + cp * 256;
    const int kb0 = kh2 * 56 + (l >> 1), li = l & 1;
    const uint32_t hv = lane * 16;
    const int pc = lane & 15, r4 = lane >> 4;
    const uint32_t wv0 = r4 * 2048 + ((((pc >> 1) ^ r4) & 7) * 2 + (pc & 1)) * 16;
    const uint32_t wv1 = r4 * 2048 + ((((pc >> 1) ^ (4 + r4)) & 7) * 2 + (pc & 1)) * 16;
    const uint32_t hb = lbase + l * 4096, wb = lbase + 16384 + l * 4096;
    unsigned* dst = (unsigned*)(Hf + WS_SLOTS) + (size_t)(pm * 32 + (lane & 31)) * 32 + pn;
    unsigned ready = 1u << pn;
    if (l == 0 && lane < 32) __hip_atomic_store(dst, 1u, __ATOMIC_RELAXED, __HIP_MEMORY_SCOPE_AGENT);
    int sb = s0 + 1; if (sb >= G2_NT) sb -= G2_NT;
    int sc = sb + 1; if (sc >= G2_NT) sc -= G2_NT;
    g2_issue_h(hs, hv, s0, hb);
    g2_issue_w(ws, wv0, wv1, s0, kb0, li, wb);
    g2_issue_w(ws, wv0, wv1, sb, kb0, li, wb + G2_ST);
    g2_issue_w(ws, wv0, wv1, sc, kb0, li, wb + 2 * G2_ST);
    WAIT_VM(8);
    BAR();
    g2_poll(myslots, lane, (g2_need(sb) | g2_need(sc)) << (16 * kh2), ready);
    g2_issue_h(hs, hv, sb, hb + G2_ST);
    g2_issue_h(hs, hv, sc, hb + 2 * G2_ST);
    int st = sc;
    uint32_t so = 0;
    for (int t = 0; t < G2_NT - 3; ++t) {
      ++st; if (st >= G2_NT) st -= G2_NT;
      if (l == 0) g2_poll(myslots, lane, g2_need(st) << (16 * kh2), ready);
      if (t == 0) WAIT_VM(4); else WAIT_VM(8);
      BAR();
      g2_issue_h(hs, hv, st, hb + so);
      g2_issue_w(ws, wv0, wv1, st, kb0, li, wb + so);
      so += G2_ST; if (so == G2_RING) so = 0;
    }
    WAIT_VM(8);
    BAR();
    WAIT_VM(0);
    BAR();
    if (l == 0) {
      g2_poll(myslots, lane, 0xffffffffu, ready);
      if (lane < 32) __hip_atomic_store(dst, 1u, __ATOMIC_RELAXED, __HIP_MEMORY_SCOPE_AGENT);
    }
    BAR();
    f32x4 kep[4];
#pragma unroll
    for (int f = 0; f < 4; ++f) kep[f] = *(LAS f32x4*)(xl + f * 1024);
    g2_finish(Hf, bid, wid, lane, kep, op);
    return;
  }

  uint32_t wa0[4];
  {
    const int g = lane >> 4, q = (lane >> 2) & 3, p = lane & 3;
    const int r = 4 * g + q;
#pragma unroll
    for (int f = 0; f < 4; ++f)
      wa0[f] = lbase + 16384 + r * 256 + (((4 * wn + f) ^ r) & 7) * 32 + p * 8;
  }
  uint32_t ha0[2];
#pragma unroll
  for (int a = 0; a < 2; ++a) ha0[a] = lbase + wm * 4096 + (a ^ kh2) * 1024 + lane * 16;

  f32x4 acc[4][4];
#pragma unroll
  for (int f = 0; f < 4; ++f)
#pragma unroll
    for (int a = 0; a < 4; ++a) acc[f][a] = (f32x4){0.f, 0.f, 0.f, 0.f};
  bf16x8 hf0[4], hf1[4];
  bf16x4 wl0[4], wh0[4], wl1[4], wh1[4];

  BAR();
#pragma unroll
  for (int i = 0; i < 4; ++i) hf0[i] = lds_b128(ha0[i & 1] + (i >> 1) * 2048);
#pragma unroll
  for (int i = 0; i < 4; ++i) { wl0[i] = lds_tr(wa0[i]); wh0[i] = lds_tr(wa0[i] + 4096); }
  int slot = 0;
  for (int t = 0; t < G2_NT - 1; ++t) {
    const uint32_t delta = (slot == 2) ? (uint32_t)(-2 * G2_ST) : (uint32_t)G2_ST;
    slot = (slot == 2) ? 0 : slot + 1;
    g2_kb<true, true>(ha0, wa0, 8192, delta, acc, hf0, wl0, wh0, hf1, wl1, wh1);
    WAIT_LGKM0();
    BAR();
    g2_kb<true, false>(ha0, wa0, 0, 0, acc, hf1, wl1, wh1, hf0, wl0, wh0);
  }
  g2_kb<true, false>(ha0, wa0, 8192, 0, acc, hf0, wl0, wh0, hf1, wl1, wh1);
  g2_kb<false, false>(ha0, wa0, 0, 0, acc, hf1, wl1, wh1, hf0, wl0, wh0);
  SCHED();
  asm volatile("s_nop 15" ::: "memory");
  SCHED();
  {
    unsigned char* xs = Hf + WS_XCH + (size_t)(pbid * 8 + wid) * 4096 + lane * 16;
#pragma unroll
    for (int f = 0; f < 4; ++f) {
      st16(xs + f * 1024, __builtin_bit_cast(u32x4, acc[f][1]));
      st16(xs + 16384 + f * 1024, __builtin_bit_cast(u32x4, acc[f][3]));
    }
  }
#pragma unroll
  for (int f = 0; f < 4; ++f) *(LAS f32x4*)(xl + f * 1024) = acc[f][2];
  WAIT_LGKM0();
  BAR();
  WAIT_VM(0);
  if (lane == 0) __hip_atomic_store((unsigned*)(Hf + WS_FLAGS + (size_t)(pbid * 4 + wid) * 64), 1u, __ATOMIC_RELAXED, __HIP_MEMORY_SCOPE_AGENT);
  {
    f32x4 kep[4];
#pragma unroll
    for (int f = 0; f < 4; ++f) kep[f] = acc[f][0];
    g2_finish(Hf, bid, wid, lane, kep, op);
  }
}

extern "C" void kernel_launch(void* const* d_in, const int* in_sizes, int n_in,
                              void* d_out, int out_size, void* d_ws, size_t ws_size,
                              hipStream_t stream) {
  const unsigned char* x = (const unsigned char*)d_in[0];
  const unsigned char* wg = (const unsigned char*)d_in[1];
  const unsigned char* wu = (const unsigned char*)d_in[2];
  const unsigned char* wd = (const unsigned char*)d_in[3];
  ffn_kernel<<<256, 512, 0, stream>>>(x, wg, wu, wd, (unsigned char*)d_out, (unsigned char*)d_ws);
}
```

```cpp
#include <hip/hip_runtime.h>
#include <hip/hip_bf16.h>
#include <stdint.h>

typedef __bf16 bf16x8 __attribute__((ext_vector_type(8)));
typedef __bf16 bf16x4 __attribute__((ext_vector_type(4)));
typedef __bf16 bf16x2 __attribute__((ext_vector_type(2)));
typedef float f32x4 __attribute__((ext_vector_type(4)));
typedef float f32x2 __attribute__((ext_vector_type(2)));
typedef unsigned u32x2 __attribute__((ext_vector_type(2)));
typedef unsigned u32x4 __attribute__((ext_vector_type(4)));
#define LAS __attribute__((address_space(3)))

#define D_ 1024
#define F_ 3584

__device__ __forceinline__ void glds16(const void* sbase, uint32_t voff, uint32_t lds_addr) {
  asm volatile("s_mov_b32 m0, %2\n\ts_nop 0\n\tglobal_load_lds_dwordx4 %0, %1"
               :: "v"(voff), "s"(sbase), "s"(lds_addr) : "memory");
}
template <int OFF>
__device__ __forceinline__ void glds16_sc1(const void* sbase, uint32_t voff, uint32_t lds_addr) {
  asm volatile("s_mov_b32 m0, %2\n\ts_nop 0\n\tglobal_load_lds_dwordx4 %0, %1 offset:%3 sc1"
               :: "v"(voff), "s"(sbase), "s"(lds_addr), "n"(OFF) : "memory");
}
__device__ __forceinline__ void l2pf(const char* addr, uint32_t lds_scratch) {
  asm volatile("s_mov_b32 m0, %1\n\ts_nop 0\n\tglobal_load_lds_dword %0, off"
               :: "v"(addr), "s"(lds_scratch) : "memory");
}
__device__ __forceinline__ void st16(unsigned char* p, u32x4 v) {
  asm volatile("global_store_dwordx4 %0, %1, off sc1\n\ts_nop 1" :: "v"(p), "v"(v) : "memory");
}
#define WAIT_VM(n) asm volatile("s_waitcnt vmcnt(" #n ")" ::: "memory")
#define WAIT_LGKM0() asm volatile("s_waitcnt lgkmcnt(0)" ::: "memory")
#define BAR() do { asm volatile("" ::: "memory"); __builtin_amdgcn_s_barrier(); asm volatile("" ::: "memory"); } while (0)
#define SCHED() __builtin_amdgcn_sched_barrier(0)

__device__ __forceinline__ bf16x8 lds_b128(uint32_t addr) { return *(LAS bf16x8*)(addr); }
__device__ __forceinline__ bf16x4 lds_tr(uint32_t a) { return __builtin_amdgcn_ds_read_tr16_b64_v4bf16((LAS bf16x4*)(a)); }
__device__ __forceinline__ bf16x8 cat8(bf16x4 lo, bf16x4 hi) { return __builtin_shufflevector(lo, hi, 0, 1, 2, 3, 4, 5, 6, 7); }
__device__ __forceinline__ f32x4 mfma16(bf16x8 a, bf16x8 b, f32x4 c) {
  return __builtin_amdgcn_mfma_f32_16x16x32_bf16(a, b, c, 0, 0, 0);
}
__device__ __forceinline__ unsigned pack2v(f32x2 v) {
  return __builtin_bit_cast(unsigned, __builtin_convertvector(v, bf16x2));
}

constexpr size_t WS_FLAGS = (size_t)16 << 20;
constexpr size_t WS_SLOTS = (size_t)17 << 20;
constexpr size_t WS_XCH = (size_t)32 << 20;

constexpr int G1_XB = 32768;
constexpr int G1_WB = 28672;
constexpr int G1_WOFF = 3 * G1_XB;
constexpr int G1_SCR = 3 * G1_XB + 2 * G1_WB;
constexpr int G1_LDS = G1_SCR + 256;
constexpr int G1_PD = 4;
constexpr int G1_NT = D_ / 64;
constexpr int CODE_PF_STRIDE = 92;

struct G1Ctx {
  const char* xs;
  const char* ws[4];
  uint32_t xv[2];
  uint32_t wv[4];
  uint32_t xl;
  uint32_t wl[4];
  uint32_t xa[2];
  uint32_t wa[2];
  const char* pf;
  const char* pf2;
  uint32_t pfs1, pfs2;
  uint32_t scr;
};

__device__ __forceinline__ void g1_issue_x(const G1Ctx& c, int idx, int t, uint32_t xoff) {
  glds16(c.xs + (idx >> 1) * (16 * 2048) + t * 128, c.xv[idx & 1], c.xl + xoff + idx * 1024);
}
__device__ __forceinline__ void g1_issue_w(const G1Ctx& c, int j, int t, uint32_t woff) {
  glds16(c.ws[j] + (size_t)t * (64 * F_ * 2), c.wv[j], c.wl[j] + woff);
}

template <int NF>
__device__ __forceinline__ void g1_tile(const G1Ctx& c, int t, uint32_t xo_cur, uint32_t xo_nxt, uint32_t xo_free, uint32_t wo_cur,
                                        f32x4 (&acc)[NF][4], bf16x8 (&xf)[2][4], bf16x4 (&wlo)[4], bf16x4 (&whi)[4]) {
  constexpr int NWL = (NF == 8) ? 3 : 4;
  constexpr int NS = 2 * NF;
  const int t1 = (t + 1 < G1_NT) ? t + 1 : G1_NT - 1;
  const int t2 = (t + 2 < G1_NT) ? t + 2 : G1_NT - 1;
  const int tp = (t + G1_PD < G1_NT) ? t + G1_PD : G1_NT - 1;
  const uint32_t wo_nxt = G1_WB - wo_cur;
  const uint32_t xa1 = c.xa[1] + xo_cur;
  const uint32_t xa0n = c.xa[0] + xo_nxt;
  const uint32_t wac0 = c.wa[0] + wo_cur, wac1 = c.wa[1] + wo_cur;
  const uint32_t wan0 = c.wa[0] + wo_nxt, wan1 = c.wa[1] + wo_nxt;
#pragma unroll
  for (int j = 0; j < NS; ++j) {
    const int s = j / NF, f = j % NF;
    if (j == NS - 2) {
      WAIT_LGKM0();
      WAIT_VM(6);
      BAR();
    }
    uint32_t a0, a1;
    if (j + 2 < NS) {
      const int s2 = (j + 2) / NF, f2 = (j + 2) % NF;
      const uint32_t o = s2 * (32 * 224) + (f2 & 1) * 14336 + (f2 >> 1) * 32;
      a0 = wac0 + o; a1 = wac1 + o;
    } else {
      const int f2 = j + 2 - NS;
      const uint32_t o = (f2 & 1) * 14336 + (f2 >> 1) * 32;
      a0 = wan0 + o; a1 = wan1 + o;
    }
    const bf16x8 w = cat8(wlo[j & 3], whi[j & 3]);
    acc[f][0] = mfma16(w, xf[s][0], acc[f][0]);
    SCHED();
    wlo[(j + 2) & 3] = lds_tr(a0);
    SCHED();
    acc[f][1] = mfma16(w, xf[s][1], acc[f][1]);
    SCHED();
    whi[(j + 2) & 3] = lds_tr(a1);
    SCHED();
    acc[f][2] = mfma16(w, xf[s][2], acc[f][2]);
    SCHED();
    if (j < 4) xf[1][j] = lds_b128(xa1 + j * 2048);
    if (j >= NS - 2) {
      const int m0 = 2 * (j - (NS - 2));
      xf[0][m0] = lds_b128(xa0n + m0 * 2048);
      xf[0][m0 + 1] = lds_b128(xa0n + (m0 + 1) * 2048);
    }
    SCHED();
    acc[f][3] = mfma16(w, xf[s][3], acc[f][3]);
    SCHED();
    if (j >= NS - 2) g1_issue_w(c, j - (NS - 2), t2, wo_cur);
    if (j + 2 < NWL) g1_issue_w(c, j + 2, t1, wo_nxt);
    if (j == 2) { l2pf(c.pf + (size_t)c.pfs1 * tp, c.scr); l2pf(c.pf2 + (size_t)c.pfs2 * tp, c.scr); }
    if (j >= 3 && j < 7) g1_issue_x(c, j - 3, t2, xo_free);
    SCHED();
  }
}

__device__ __forceinline__ unsigned geglu2(f32x2 g, f32x2 u) {
  const float c1 = -2.0f * 0.7978845608028654f * 1.4426950408889634f;
  const float c2 = c1 * 0.044715f;
  f32x2 p = (g * g) * c2 + c1;
  f32x2 a = g * p;
  f32x2 e; e.x = __builtin_amdgcn_exp2f(a.x); e.y = __builtin_amdgcn_exp2f(a.y);
  f32x2 d = e + 1.0f;
  f32x2 r; r.x = __builtin_amdgcn_rcpf(d.x); r.y = __builtin_amdgcn_rcpf(d.y);
  f32x2 ge = g * r;
  return pack2v(ge * u);
}

template <int NF>
__device__ __forceinline__ void g1_epi(f32x4 (&acc)[NF][4], unsigned char* hb, int nfg0, int lane) {
  constexpr int NP = NF / 2;
  u32x2 hp[NP][4];
#pragma unroll
  for (int i = 0; i < NP; ++i) {
#pragma unroll
    for (int e = 0; e < 4; ++e) {
      hp[i][e].x = geglu2((f32x2){acc[2 * i][e][0], acc[2 * i][e][1]}, (f32x2){acc[2 * i + 1][e][0], acc[2 * i + 1][e][1]});
      hp[i][e].y = geglu2((f32x2){acc[2 * i][e][2], acc[2 * i][e][3]}, (f32x2){acc[2 * i + 1][e][2], acc[2 * i + 1][e][3]});
    }
    if (NP == 4) {
      if (i & 1) {
        unsigned char* p = hb + ((nfg0 + i) >> 1) * 8192;
#pragma unroll
        for (int m = 0; m < 4; ++m)
          st16(p + m * 1024, (u32x4){hp[i - 1][m].x, hp[i - 1][m].y, hp[i][m].x, hp[i][m].y});
      }
    } else if (i == 2) {
      const int half = nfg0 & 1;
      unsigned char* q = hb + ((nfg0 + half) >> 1) * 8192;
      unsigned char* p = hb - lane * 16 + ((nfg0 + (half ? 0 : 2)) >> 1) * 8192 + (((lane >> 5) + 2 * half) * 16 + (lane & 15)) * 16;
#pragma unroll
      for (int m = 0; m < 4; ++m) {
        const u32x2 sv = half ? hp[0][m] : hp[2][m];
        const u32x2 f0 = half ? hp[1][m] : hp[0][m], f1 = half ? hp[2][m] : hp[1][m];
        st16(q + m * 1024, (u32x4){f0.x, f0.y, f1.x, f1.y});
        const unsigned nx = __shfl_down(sv.x, 16), ny = __shfl_down(sv.y, 16);
        if ((lane & 16) == 0) st16(p + m * 1024, (u32x4){sv.x, sv.y, nx, ny});
      }
    }
  }
}

template <int NF>
__device__ __forceinline__ void g1_run(const G1Ctx& c, unsigned char* hb, int nfg0, int lane) {
  constexpr int NWL = (NF == 8) ? 3 : 4;
  f32x4 acc[NF][4];
#pragma unroll
  for (int f = 0; f < NF; ++f)
#pragma unroll
    for (int m = 0; m < 4; ++m) acc[f][m] = (f32x4){0.f, 0.f, 0.f, 0.f};
  bf16x8 xf[2][4];
  bf16x4 wlo[4], whi[4];
#pragma unroll
  for (int i = 0; i < 4; ++i) g1_issue_x(c, i, 0, 0);
#pragma unroll
  for (int i = 0; i < NWL; ++i) g1_issue_w(c, i, 0, 0);
#pragma unroll
  for (int i = 0; i < 2; ++i) g1_issue_w(c, i, 1, G1_WB);
  WAIT_VM(2);
  BAR();
#pragma unroll
  for (int m = 0; m < 4; ++m) xf[0][m] = lds_b128(c.xa[0] + m * 2048);
  wlo[0] = lds_tr(c.wa[0]); whi[0] = lds_tr(c.wa[1]);
  wlo[1] = lds_tr(c.wa[0] + 14336); whi[1] = lds_tr(c.wa[1] + 14336);
  SCHED();
#pragma unroll
  for (int i = 0; i < 4; ++i) g1_issue_x(c, i, 1, G1_XB);
  if (c.pfs1 != 0) {
#pragma unroll
    for (int i = 2; i < G1_PD; ++i) { l2pf(c.pf + (size_t)c.pfs1 * i, c.scr); l2pf(c.pf2 + (size_t)c.pfs2 * i, c.scr); }
  }
  SCHED();
  uint32_t xo0 = 0, xo1 = G1_XB, xo2 = 2 * G1_XB, wo = 0;
#pragma nounroll
  for (int t = 0; t < G1_NT; ++t) {
    g1_tile<NF>(c, t, xo0, xo1, xo2, wo, acc, xf, wlo, whi);
    const uint32_t tmp = xo0; xo0 = xo1; xo1 = xo2; xo2 = tmp;
    wo = G1_WB - wo;
  }
  g1_epi<NF>(acc, hb, nfg0, lane);
}

constexpr int G2_NT = 28;
constexpr int G2_ST = 32768;
constexpr int G2_RING = 3 * G2_ST;

__device__ __forceinline__ void g2_issue_h(const char* hs, uint32_t hv, int st, uint32_t hdst) {
  const char* hp = hs + (size_t)st * 16384;
  glds16_sc1<0>(hp, hv, hdst);
  glds16_sc1<1024>(hp, hv, hdst);
  glds16_sc1<2048>(hp, hv, hdst);
  glds16_sc1<3072>(hp, hv, hdst);
}
__device__ __forceinline__ void g2_issue_w(const char* ws, uint32_t wv0, uint32_t wv1, int st, int kb0, int i, uint32_t wdst) {
  const bool shared = ((kb0 + 2 * st) % 7) == 3;
  const char* wp = ws + (size_t)st * (64 * 2048) + (shared ? 4 * i : 16 * i) * 2048;
  const int rs = (shared ? 8 : 4) * 2048;
  glds16(wp, wv0, wdst);
  glds16(wp + rs, wv1, wdst + 1024);
  glds16(wp + 2 * rs, wv0, wdst + 2048);
  glds16(wp + 3 * rs, wv1, wdst + 3072);
}
__device__ __forceinline__ unsigned g2_need(int st) {
  const int lo = (64 * st) / 112, hi = (64 * st + 63) / 112;
  return (1u << lo) | (1u << hi);
}
__device__ __forceinline__ void g2_poll(const unsigned* myslots, int lane, unsigned need, unsigned& ready) {
  for (int spin = 0; (ready & need) != need && spin < (1 << 20); ++spin) {
    const unsigned v = __hip_atomic_load(myslots + (lane & 31), __ATOMIC_RELAXED, __HIP_MEMORY_SCOPE_AGENT);
    ready |= (unsigned)__ballot(v == 1u);
  }
  asm volatile("" ::: "memory");
}

template <bool RD, bool UPD>
__device__ __forceinline__ void g2_kb(uint32_t (&ha)[2], uint32_t (&wa)[4], uint32_t off, uint32_t delta, f32x4 (&acc)[4][4],
                                      const bf16x8 (&hc)[4], const bf16x4 (&wlc)[4], const bf16x4 (&whc)[4],
                                      bf16x8 (&hn)[4], bf16x4 (&wln)[4], bf16x4 (&whn)[4]) {
#pragma unroll
  for (int f = 0; f < 4; ++f) {
    const bf16x8 w = cat8(wlc[f], whc[f]);
#pragma unroll
    for (int a = 0; a < 4; ++a) {
      acc[f][a] = mfma16(w, hc[a], acc[f][a]);
      SCHED();
      const int g = 4 * f + a;
      if (RD) {
        if (g < 4) hn[g] = lds_b128(ha[g & 1] + off + (g >> 1) * 2048);
        else if (g < 12) { const int q = (g - 4) >> 1; if (g & 1) whn[q] = lds_tr(wa[q] + off + 4096); else wln[q] = lds_tr(wa[q] + off); }
      }
      if (UPD) {
        if (g == 12) { wa[0] += delta; wa[1] += delta; }
        if (g == 13) { wa[2] += delta; wa[3] += delta; }
        if (g == 14) { ha[0] += delta; ha[1] += delta; }
      }
      SCHED();
    }
  }
}

__device__ __forceinline__ void g2_finish(unsigned char* ws_base, int bid, int wid, int lane, f32x4 (&kep)[4], unsigned char* op) {
  const unsigned* iflag = (const unsigned*)(ws_base + WS_FLAGS + (size_t)(bid * 4 + (wid & 3)) * 64);
  for (int spin = 0; spin < (1 << 22); ++spin) {
    const unsigned v = __builtin_amdgcn_readfirstlane(__hip_atomic_load(iflag, __ATOMIC_RELAXED, __HIP_MEMORY_SCOPE_AGENT));
    if (v == 1u) break;
    __builtin_amdgcn_s_sleep(1);
  }
  asm volatile("" ::: "memory");
  const unsigned char* xr = ws_base + WS_XCH + (size_t)(bid * 8 + wid) * 4096 + lane * 16;
  f32x4 o0, o1, o2, o3;
  asm volatile("global_load_dwordx4 %0, %4, off sc1\n\t"
               "global_load_dwordx4 %1, %4, off offset:1024 sc1\n\t"
               "global_load_dwordx4 %2, %4, off offset:2048 sc1\n\t"
               "global_load_dwordx4 %3, %4, off offset:3072 sc1\n\t"
               "s_waitcnt vmcnt(0)"
               : "=&v"(o0), "=&v"(o1), "=&v"(o2), "=&v"(o3) : "v"(xr) : "memory");
  kep[0] += o0; kep[1] += o1; kep[2] += o2; kep[3] += o3;
  const bool odd = (lane & 16) != 0;
#pragma unroll
  for (int h = 0; h < 2; ++h) {
    u32x2 p0, p1;
    p0.x = pack2v((f32x2){kep[2 * h][0], kep[2 * h][1]}); p0.y = pack2v((f32x2){kep[2 * h][2], kep[2 * h][3]});
    p1.x = pack2v((f32x2){kep[2 * h + 1][0], kep[2 * h + 1][1]}); p1.y = pack2v((f32x2){kep[2 * h + 1][2], kep[2 * h + 1][3]});
    const unsigned rx = __shfl_xor(odd ? p0.x : p1.x, 16), ry = __shfl_xor(odd ? p0.y : p1.y, 16);
    const u32x4 v = odd ? (u32x4){rx, ry, p1.x, p1.y} : (u32x4){p0.x, p0.y, rx, ry};
    *(u32x4*)(op + h * 64 + (odd ? 24 : 0)) = v;
  }
}

__global__ __launch_bounds__(512, 2) void ffn_kernel(const unsigned char* __restrict__ x, const unsigned char* __restrict__ wg,
                                                     const unsigned char* __restrict__ wu, const unsigned char* __restrict__ wd,
                                                     unsigned char* __restrict__ out, unsigned char* __restrict__ Hf) {
  __shared__ __attribute__((aligned(1024))) unsigned char smem[G1_LDS];
  const uint32_t lbase = (uint32_t)(uintptr_t)(LAS unsigned char*)smem;
  if (__builtin_amdgcn_readfirstlane(threadIdx.x) >= 320)
    l2pf((const char*)__builtin_amdgcn_s_getpc() + (size_t)(threadIdx.x - 320) * CODE_PF_STRIDE, lbase + G1_SCR);
  SCHED();
  const int bid = blockIdx.x;
  const int xcd = bid & 7, idx = bid >> 3;
  const int pm = 4 * (xcd & 1) + (idx & 3), pn = 8 * (xcd >> 1) + (idx >> 2);
  const int brow = pm * 256;
  const int lane = threadIdx.x & 63;
  const int wid = __builtin_amdgcn_readfirstlane(threadIdx.x >> 6);
  const int wr = wid & 3, wc = wid >> 2;

  const char* pfa; const char* pfb; uint32_t pfbs;
  {
    const int li = idx * 56 + (lane < 56 ? lane : 0);
    const int mat = li / 896, r = (li % 896) / 14, cc = li % 14;
    pfa = (const char*)(mat ? wu : wg) + (size_t)r * (F_ * 2) + (xcd >> 1) * 1792 + cc * 128;
    const char* px = (const char*)x + (size_t)(brow + 32 * (pn & 7) + (lane & 31)) * 2048;
    if (wid < 4) {
      if (wid & 1) l2pf(pfa + (size_t)(64 * F_ * 2) * (wid >> 1), lbase + G1_SCR);
      else l2pf(px + 128 * (wid >> 1), lbase + G1_SCR);
    }
    if (lane >= 32 && lane < 48) {
      pfb = (const char*)wd + (size_t)((bid * 16 + (lane - 32)) * 128) - (size_t)2 * (256 * 2048);
      pfbs = 256 * 2048;
    } else {
      pfb = px;
      pfbs = 128;
    }
  }
  unsigned* myslots = (unsigned*)(Hf + WS_SLOTS) + (size_t)(pm * 32 + pn) * 32;
  if (wid == 4) {
    if (lane < 32) __hip_atomic_store(myslots + lane, 0u, __ATOMIC_RELAXED, __HIP_MEMORY_SCOPE_AGENT);
    else if (lane < 36) __hip_atomic_store((unsigned*)(Hf + WS_FLAGS + (size_t)(bid * 4 + lane - 32) * 64), 0u, __ATOMIC_RELAXED, __HIP_MEMORY_SCOPE_AGENT);
  }

  {
    G1Ctx c;
    c.xs = (const char*)x + (size_t)(brow + 32 * wid) * 2048;
#pragma unroll
    for (int a = 0; a < 2; ++a) {
      const int r = 8 * a + (lane >> 3);
      const int ch = (lane & 7) ^ ((4 * a + (lane >> 4)) & 7);
      c.xv[a] = r * 2048 + ch * 16;
    }
    c.xl = lbase + wid * 4096;
    const int e0 = (wid < 4) ? 3 * wid : 12 + 4 * (wid - 4);
#pragma unroll
    for (int j = 0; j < 4; ++j) {
      int e = e0 + j; if (e > 27) e = 27;
      const int mat = e / 14, jj = e % 14;
      const int ci = 64 * jj + lane;
      const int rowp = ci / 14, cc = ci % 14;
      const int row = rowp ^ (((rowp >> 3) & 1) << 2);
      c.wv[j] = row * (F_ * 2) + cc * 16;
      c.ws[j] = (const char*)(mat ? wu : wg) + pn * 224;
      c.wl[j] = lbase + G1_WOFF + mat * 14336 + jj * 1024;
    }
    c.pf = pfa; c.pf2 = pfb; c.pfs1 = 64 * F_ * 2; c.pfs2 = pfbs;
    if (wid != 4) { c.pf = (const char*)x; c.pf2 = (const char*)x; c.pfs1 = 0; c.pfs2 = 0; }
    c.scr = lbase + G1_SCR;
    const int g = lane >> 4, r16 = lane & 15;
#pragma unroll
    for (int s = 0; s < 2; ++s)
      c.xa[s] = lbase + (wr * 64 + r16) * 128 + (((4 * s + g) ^ (r16 >> 1)) & 7) * 16;
    const int q = r16 >> 2, p = lane & 3;
    const int fbase = (wc == 0) ? ((pn & 1) ? 3 : 0) : ((pn & 1) ? 0 : 4);
#pragma unroll
    for (int i = 0; i < 2; ++i)
      c.wa[i] = lbase + G1_WOFF + (8 * g + 4 * (i ^ (g & 1)) + q) * 224 + fbase * 32 + p * 8;
    const int nfg0 = 7 * pn + fbase;
    unsigned char* hb = Hf + (size_t)(2 * pm + (wr >> 1)) * (112 * 8192) + (wr & 1) * 4096 + lane * 16;
    if (wc == 0) g1_run<8>(c, hb, nfg0, lane);
    else g1_run<6>(c, hb, nfg0, lane);
  }
  WAIT_LGKM0();
  WAIT_VM(0);
  BAR();

  const int kh2 = pn >> 4, cp = ((pn >> 3) & 1) * 4 + ((pn >> 1) & 3), rp = 2 * pm + (pn & 1);
  const int s0 = ((112 * (pn & 15) + 63) / 64) % G2_NT;
  const int pbid = bid ^ 4;
  const int wm = wr & 1, wn = wr >> 1;
  unsigned char* op = out + (size_t)(rp * 128 + wm * 64 + wc * 32 + kh2 * 16 + (lane & 15)) * 2048 + cp * 256 + wn * 128 + (lane >> 4) * 8;
  const uint32_t xl = lbase + G2_RING + wr * 4096 + lane * 16;

  if (wc == 1) {
    const int l = wr;
    const char* hs = (const char*)Hf + (size_t)(rp * 112 + kh2 * 56) * 8192 + l * 4096;
    const char* ws = (const char*)wd + (size_t)(kh2 * 1792 + 32 * (l >> 1)) * 2048 + cp * 256;
    const int kb0 = kh2 * 56 + (l >> 1), li = l & 1;
    const uint32_t hv = lane * 16;
    const int pc = lane & 15, r4 = lane >> 4;
    const uint32_t wv0 = r4 * 2048 + ((((pc >> 1) ^ r4) & 7) * 2 + (pc & 1)) * 16;
    const uint32_t wv1 = r4 * 2048 + ((((pc >> 1) ^ (4 + r4)) & 7) * 2 + (pc & 1)) * 16;
    const uint32_t hb = lbase + l * 4096, wb = lbase + 16384 + l * 4096;
    unsigned* dst = (unsigned*)(Hf + WS_SLOTS) + (size_t)(pm * 32 + (lane & 31)) * 32 + pn;
    unsigned ready = 1u << pn;
    if (l == 0 && lane < 32) __hip_atomic_store(dst, 1u, __ATOMIC_RELAXED, __HIP_MEMORY_SCOPE_AGENT);
    int sb = s0 + 1; if (sb >= G2_NT) sb -= G2_NT;
    int sc = sb + 1; if (sc >= G2_NT) sc -= G2_NT;
    g2_issue_h(hs, hv, s0, hb);
    g2_issue_w(ws, wv0, wv1, s0, kb0, li, wb);
    g2_issue_w(ws, wv0, wv1, sb, kb0, li, wb + G2_ST);
    g2_issue_w(ws, wv0, wv1, sc, kb0, li, wb + 2 * G2_ST);
    WAIT_VM(8);
    BAR();
    g2_poll(myslots, lane, (g2_need(sb) | g2_need(sc)) << (16 * kh2), ready);
    g2_issue_h(hs, hv, sb, hb + G2_ST);
    g2_issue_h(hs, hv, sc, hb + 2 * G2_ST);
    int st = sc;
    uint32_t so = 0;
    for (int t = 0; t < G2_NT - 3; ++t) {
      ++st; if (st >= G2_NT) st -= G2_NT;
      if (l == 0) g2_poll(myslots, lane, g2_need(st) << (16 * kh2), ready);
      if (t == 0) WAIT_VM(4); else WAIT_VM(8);
      BAR();
      g2_issue_h(hs, hv, st, hb + so);
      g2_issue_w(ws, wv0, wv1, st, kb0, li, wb + so);
      so += G2_ST; if (so == G2_RING) so = 0;
    }
    WAIT_VM(8);
    BAR();
    WAIT_VM(0);
    BAR();
    if (l == 0) {
      g2_poll(myslots, lane, 0xffffffffu, ready);
      if (lane < 32) __hip_atomic_store(dst, 1u, __ATOMIC_RELAXED, __HIP_MEMORY_SCOPE_AGENT);
    }
    BAR();
    f32x4 kep[4];
#pragma unroll
    for (int f = 0; f < 4; ++f) kep[f] = *(LAS f32x4*)(xl + f * 1024);
    g2_finish(Hf, bid, wid, lane, kep, op);
    return;
  }

  uint32_t wa0[4];
  {
    const int g = lane >> 4, q = (lane >> 2) & 3, p = lane & 3;
    const int r = 4 * g + q;
#pragma unroll
    for (int f = 0; f < 4; ++f)
      wa0[f] = lbase + 16384 + r * 256 + (((4 * wn + f) ^ r) & 7) * 32 + p * 8;
  }
  uint32_t ha0[2];
#pragma unroll
  for (int a = 0; a < 2; ++a) ha0[a] = lbase + wm * 4096 + (a ^ kh2) * 1024 + lane * 16;

  f32x4 acc[4][4];
#pragma unroll
  for (int f = 0; f < 4; ++f)
#pragma unroll
    for (int a = 0; a < 4; ++a) acc[f][a] = (f32x4){0.f, 0.f, 0.f, 0.f};
  bf16x8 hf0[4], hf1[4];
  bf16x4 wl0[4], wh0[4], wl1[4], wh1[4];

  BAR();
#pragma unroll
  for (int i = 0; i < 4; ++i) hf0[i] = lds_b128(ha0[i & 1] + (i >> 1) * 2048);
#pragma unroll
  for (int i = 0; i < 4; ++i) { wl0[i] = lds_tr(wa0[i]); wh0[i] = lds_tr(wa0[i] + 4096); }
  int slot = 0;
  for (int t = 0; t < G2_NT - 1; ++t) {
    const uint32_t delta = (slot == 2) ? (uint32_t)(-2 * G2_ST) : (uint32_t)G2_ST;
    slot = (slot == 2) ? 0 : slot + 1;
    g2_kb<true, true>(ha0, wa0, 8192, delta, acc, hf0, wl0, wh0, hf1, wl1, wh1);
    WAIT_LGKM0();
    BAR();
    g2_kb<true, false>(ha0, wa0, 0, 0, acc, hf1, wl1, wh1, hf0, wl0, wh0);
  }
  g2_kb<true, false>(ha0, wa0, 8192, 0, acc, hf0, wl0, wh0, hf1, wl1, wh1);
  g2_kb<false, false>(ha0, wa0, 0, 0, acc, hf1, wl1, wh1, hf0, wl0, wh0);
  SCHED();
  asm volatile("s_nop 15" ::: "memory");
  SCHED();
  {
    unsigned char* xs = Hf + WS_XCH + (size_t)(pbid * 8 + wid) * 4096 + lane * 16;
#pragma unroll
    for (int f = 0; f < 4; ++f) {
      st16(xs + f * 1024, __builtin_bit_cast(u32x4, acc[f][1]));
      st16(xs + 16384 + f * 1024, __builtin_bit_cast(u32x4, acc[f][3]));
    }
  }
#pragma unroll
  for (int f = 0; f < 4; ++f) *(LAS f32x4*)(xl + f * 1024) = acc[f][2];
  WAIT_LGKM0();
  BAR();
  WAIT_VM(0);
  if (lane == 0) __hip_atomic_store((unsigned*)(Hf + WS_FLAGS + (size_t)(pbid * 4 + wid) * 64), 1u, __ATOMIC_RELAXED, __HIP_MEMORY_SCOPE_AGENT);
  {
    f32x4 kep[4];
#pragma unroll
    for (int f = 0; f < 4; ++f) kep[f] = acc[f][0];
    g2_finish(Hf, bid, wid, lane, kep, op);
  }
}

extern "C" void kernel_launch(void* const* d_in, const int* in_sizes, int n_in,
                              void* d_out, int out_size, void* d_ws, size_t ws_size,
                              hipStream_t stream) {
  const unsigned char* x = (const unsigned char*)d_in[0];
  const unsigned char* wg = (const unsigned char*)d_in[1];
  const unsigned char* wu = (const unsigned char*)d_in[2];
  const unsigned char* wd = (const unsigned char*)d_in[3];
  ffn_kernel<<<256, 512, 0, stream>>>(x, wg, wu, wd, (unsigned char*)d_out, (unsigned char*)d_ws);
}
```
